```python
import torch

def _softmax(x, axis=-1):
    return torch.softmax(x, dim=axis)
def _gelu(x):
    return torch.nn.functional.gelu(x, approximate="tanh")
def _silu(x):
    return torch.nn.functional.silu(x)
def _sigmoid(x):
    return torch.sigmoid(x)
def _layernorm(x, axis=-1, eps=1e-5):
    m = x.mean(dim=axis, keepdim=True)
    v = x.var(dim=axis, keepdim=True, unbiased=False)
    return (x - m) / torch.sqrt(v + eps)
def _scatter_add(tgt, idx, src, axis=0):
    return tgt.clone().index_add_(axis, idx, src)
def _segment_sum(src, seg, n):
    out = torch.zeros((n,) + src.shape[1:], dtype=src.dtype)
    return out.index_add_(0, seg, src)
def _pad(x, pw):
    flat = tuple(v for pair in reversed(pw) for v in pair)
    return torch.nn.functional.pad(x, flat)

kernel_name = 'gated_mix__linear_kernel_hedgehog__mixer_token_mlp__prompt'

def setup_inputs():

    _w = (lambda a: (lambda t: t.to(torch.bfloat16) if t.is_floating_point() else t)(torch.as_tensor(a)))
    def _A_setup():
        B, S, D, H = 1, 2048, 1024, 32
        Dh = D // H
        return {
            "x": _w((lambda *s: torch.randn(*s, dtype=torch.bfloat16) * 0.05)(B, S, D)),
            "wqkv": _w((lambda *s: torch.randn(*s, dtype=torch.bfloat16) * 0.8)(D, 3 * D)),
            "w_phi": _w((lambda *s: torch.randn(*s, dtype=torch.bfloat16) * 0.8)(H, Dh, Dh) * (Dh ** -0.5)),
            "wo": _w((lambda *s: torch.randn(*s, dtype=torch.bfloat16) * 0.05)(D, D)),
        }
    _A_in = _A_setup()

    def _B_setup():
        B, S, D, F = 1, 2048, 1024, 3584
        return {
            "x": _w((lambda *s: torch.randn(*s, dtype=torch.bfloat16) * 0.05)(B, S, D)),
            "w1": _w((lambda *s: torch.randn(*s, dtype=torch.bfloat16) * 0.8)(S, F) * (S ** -0.5)),
            "b1": _w((lambda *s: torch.randn(*s, dtype=torch.bfloat16) * 12.8)(F) * 0.02),
            "w2": _w((lambda *s: torch.randn(*s, dtype=torch.bfloat16) * 0.05)(F, S) * (F ** -0.5)),
            "b2": _w((lambda *s: torch.randn(*s, dtype=torch.bfloat16) * 0.8)(S) * 0.02),
        }
    _B_in = _B_setup()

    return {
        "A_x": _A_in["x"],
        "A_wqkv": _A_in["wqkv"],
        "A_w_phi": _A_in["w_phi"],
        "A_wo": _A_in["wo"],
        "B_x": _B_in["x"],
        "B_w1": _B_in["w1"],
        "B_b1": _B_in["b1"],
        "B_w2": _B_in["w2"],
        "B_b2": _B_in["b2"],
        "gate_w": _w((lambda *s: torch.randn(*s, dtype=torch.bfloat16) * 0.05)(1024) * 16.0),
    }

def reference(A_x, A_wqkv, A_w_phi, A_wo, B_x, B_w1, B_b1, B_w2, B_b2, gate_w):
    def _A_ref(x, wqkv, w_phi, wo):
        B, S, D = x.shape
        H = 32; Dh = D // H
        qkv = (lambda x, *d: x.permute(*d))((x @ wqkv).reshape(B, S, 3, H, Dh), 2, 0, 3, 1, 4)
        q, k, v = qkv[0], qkv[1], qkv[2]
        pq = torch.einsum("bhsd,hdf->bhsf", q, w_phi)
        pk = torch.einsum("bhsd,hdf->bhsf", k, w_phi)
        phi_q = torch.concatenate([_softmax(pq, axis=-1), _softmax(-pq, axis=-1)], axis=-1)
        phi_k = torch.concatenate([_softmax(pk, axis=-1), _softmax(-pk, axis=-1)], axis=-1)
        kv = torch.einsum("bhsf,bhsd->bhfd", phi_k, v)
        z  = torch.einsum("bhsf->bhf", phi_k)
        num = torch.einsum("bhqf,bhfd->bhqd", phi_q, kv)
        den = torch.einsum("bhqf,bhf->bhq", phi_q, z)[..., None] + 1e-6
        o = num / den
        return (lambda x, *d: x.permute(*d))(o, 0, 2, 1, 3).reshape(B, S, D) @ wo

    def _B_ref(x, w1, b1, w2, b2):
        B, S, D = x.shape
        h = _layernorm(x, axis=-1)
        u = torch.einsum("bsd,sf->bdf", h, w1) + b1
        u = _gelu(u)
        y = torch.einsum("bdf,fs->bds", u, w2) + b2
        return (lambda x, *d: x.permute(*d))(y, 0, 2, 1)

    x = A_x
    g = _sigmoid((x * gate_w).sum(-1, keepdims=True))
    a = _A_ref(x, A_wqkv, A_w_phi, A_wo)
    b = _B_ref(x, B_w1, B_b1, B_w2, B_b2)
    return g * a + (1 - g) * b
```

```cpp
#include <hip/hip_runtime.h>
#include <hip/hip_bf16.h>
typedef unsigned short u16;
__device__ inline float bf2f(u16 b){ return __uint_as_float(((unsigned)b)<<16); }
__device__ inline u16 f2bf(float f){ unsigned u=__float_as_uint(f); u+=0x7fffu+((u>>16)&1u); return (u16)(u>>16); }
constexpr int S=2048,D=1024,H=32,DH=32,F=3584;

__global__ void gemm_naive(const u16* A,long sam,long sak,const u16* B,long sbk,long sbn,float* C,int M,int N,int K){
  __shared__ float As[16][17],Bs[16][17];
  int tx=threadIdx.x&15, ty=threadIdx.x>>4;
  int m=blockIdx.y*16+ty, n=blockIdx.x*16+tx;
  float acc=0;
  for(int k0=0;k0<K;k0+=16){
    As[ty][tx]=bf2f(A[(long)(blockIdx.y*16+ty)*sam+(long)(k0+tx)*sak]);
    Bs[ty][tx]=bf2f(B[(long)(k0+ty)*sbk+(long)(blockIdx.x*16+tx)*sbn]);
    __syncthreads();
    for(int k=0;k<16;k++) acc+=As[ty][k]*Bs[k][tx];
    __syncthreads();
  }
  C[(long)m*N+n]=acc;
}
__global__ void ln_gate(const u16* bx,const u16* ax,const u16* gw,u16* h,float* g){
  int s=blockIdx.x; int t=threadIdx.x;
  __shared__ float red[256];
  float v[4]; float sum=0;
  for(int i=0;i<4;i++){v[i]=bf2f(bx[s*D+t+256*i]); sum+=v[i];}
  red[t]=sum; __syncthreads();
  for(int o=128;o>0;o>>=1){ if(t<o) red[t]+=red[t+o]; __syncthreads(); }
  float mean=red[0]/D; __syncthreads();
  float q=0; for(int i=0;i<4;i++){float d=v[i]-mean; q+=d*d;}
  red[t]=q; __syncthreads();
  for(int o=128;o>0;o>>=1){ if(t<o) red[t]+=red[t+o]; __syncthreads(); }
  float var=red[0]/D; __syncthreads();
  float r=1.0f/sqrtf(var+1e-5f);
  for(int i=0;i<4;i++) h[s*D+t+256*i]=f2bf((v[i]-mean)*r);
  float z=0; for(int i=0;i<4;i++) z+=bf2f(ax[s*D+t+256*i])*bf2f(gw[t+256*i]);
  red[t]=z; __syncthreads();
  for(int o=128;o>0;o>>=1){ if(t<o) red[t]+=red[t+o]; __syncthreads(); }
  if(t==0) g[s]=1.0f/(1.0f+expf(-red[0]));
}
__global__ void phi_k(const float* qkv,const u16* wphi,float* phiq,float* phik){
  int s=blockIdx.x, h=threadIdx.x>>5, f=threadIdx.x&31;
  for(int which=0;which<2;which++){
    const float* q=qkv+(long)s*3*D+which*D+h*DH;
    float p=0; for(int d=0;d<DH;d++) p+=bf2f(f2bf(q[d]))*bf2f(wphi[(h*DH+d)*DH+f]);
    float mx=p,mn=p;
    for(int o=16;o>0;o>>=1){ mx=fmaxf(mx,__shfl_xor(mx,o,32)); mn=fminf(mn,__shfl_xor(mn,o,32)); }
    float e1=expf(p-mx), e2=expf(mn-p); float s1=e1,s2=e2;
    for(int o=16;o>0;o>>=1){ s1+=__shfl_xor(s1,o,32); s2+=__shfl_xor(s2,o,32); }
    float* out=(which?phik:phiq)+((long)s*H+h)*64;
    out[f]=e1/s1; out[32+f]=e2/s2;
  }
}
__global__ void kv_k(const float* qkv,const float* phik,float* kv,float* z){
  int h=blockIdx.x; int f=threadIdx.x>>5, d=threadIdx.x&31;
  for(int ff=f;ff<64;ff+=32){
    float acc=0, zz=0;
    for(int s=0;s<S;s++){ float p=phik[((long)s*H+h)*64+ff]; acc+=p*bf2f(f2bf(qkv[(long)s*3*D+2*D+h*DH+d])); zz+=p; }
    kv[(h*64+ff)*32+d]=acc; if(d==0) z[h*64+ff]=zz;
  }
}
__global__ void o_k(const float* phiq,const float* kv,const float* z,u16* o){
  int s=blockIdx.x, h=threadIdx.x>>5, d=threadIdx.x&31;
  float num=0,den=0;
  for(int f=0;f<64;f++){ float p=phiq[((long)s*H+h)*64+f]; num+=p*kv[(h*64+f)*32+d]; den+=p*z[h*64+f]; }
  o[(long)s*D+h*DH+d]=f2bf(num/(den+1e-6f));
}
__global__ void gelu_k(const float* u,const u16* b1,u16* uo){
  long i=(long)blockIdx.x*256+threadIdx.x; int f=i%F;
  float x=u[i]+bf2f(b1[f]);
  float t=tanhf(0.7978845608028654f*(x+0.044715f*x*x*x));
  uo[i]=f2bf(0.5f*x*(1.0f+t));
}
__global__ void final_k(const float* a,const float* y,const u16* b2,const float* g,u16* out){
  long i=(long)blockIdx.x*256+threadIdx.x; int s=i/D, d=i%D;
  float b=y[(long)d*S+s]+bf2f(b2[s]);
  out[i]=f2bf(g[s]*a[i]+(1.0f-g[s])*b);
}
extern "C" void kernel_launch(void* const* d_in,const int* in_sizes,int n_in,void* d_out,int out_size,void* d_ws,size_t ws_size,hipStream_t stream){
  const u16* ax=(const u16*)d_in[0]; const u16* wqkv=(const u16*)d_in[1]; const u16* wphi=(const u16*)d_in[2]; const u16* wo=(const u16*)d_in[3];
  const u16* bx=(const u16*)d_in[4]; const u16* w1=(const u16*)d_in[5]; const u16* b1=(const u16*)d_in[6]; const u16* w2=(const u16*)d_in[7]; const u16* b2=(const u16*)d_in[8]; const u16* gw=(const u16*)d_in[9];
  char* ws=(char*)d_ws; size_t off=0;
  auto alloc=[&](size_t b){ void* p=ws+off; off+=(b+255)/256*256; return p; };
  u16* h=(u16*)alloc((size_t)S*D*2); float* g=(float*)alloc(S*4);
  float* qkv=(float*)alloc((size_t)S*3*D*4);
  float* phiq=(float*)alloc((size_t)S*H*64*4); float* phik=(float*)alloc((size_t)S*H*64*4);
  float* kv=(float*)alloc(H*64*32*4); float* z=(float*)alloc(H*64*4);
  u16* o=(u16*)alloc((size_t)S*D*2); float* a=(float*)alloc((size_t)S*D*4);
  float* u=(float*)alloc((size_t)D*F*4); u16* ub=(u16*)alloc((size_t)D*F*2); float* y=(float*)alloc((size_t)D*S*4);
  ln_gate<<<S,256,0,stream>>>(ax,ax,gw,h,g);
  gemm_naive<<<dim3(3*D/16,S/16),256,0,stream>>>(ax,D,1,wqkv,3*D,1,qkv,S,3*D,D);
  phi_k<<<S,1024,0,stream>>>(qkv,wphi,phiq,phik);
  kv_k<<<H,1024,0,stream>>>(qkv,phik,kv,z);
  o_k<<<S,1024,0,stream>>>(phiq,kv,z,o);
  gemm_naive<<<dim3(D/16,S/16),256,0,stream>>>(o,D,1,wo,D,1,a,S,D,D);
  gemm_naive<<<dim3(F/16,D/16),256,0,stream>>>(h,1,D,w1,F,1,u,D,F,S);
  gelu_k<<<(D*F)/256,256,0,stream>>>(u,b1,ub);
  gemm_naive<<<dim3(S/16,D/16),256,0,stream>>>(ub,F,1,w2,S,1,y,D,S,F);
  final_k<<<(S*D)/256,256,0,stream>>>(a,y,b2,g,(u16*)d_out);
}
```

```cpp
#include <hip/hip_runtime.h>
#include <hip/hip_bf16.h>
#include <type_traits>
#include <utility>

#define LAS __attribute__((address_space(3)))
#define DI __device__ __forceinline__
#define IC(n) std::integral_constant<int,(n)>{}
typedef unsigned short u16;
typedef float f32x4 __attribute__((ext_vector_type(4)));
typedef float f32x2 __attribute__((ext_vector_type(2)));
typedef int i32x4 __attribute__((ext_vector_type(4)));
typedef unsigned u32x4 __attribute__((ext_vector_type(4)));
typedef unsigned u32x2 __attribute__((ext_vector_type(2)));
typedef short bf16x8 __attribute__((ext_vector_type(8)));
typedef __bf16 bf16x2_t __attribute__((ext_vector_type(2)));

constexpr int S=2048, D=1024, F=3584;
constexpr size_t WS_H   = 0;
constexpr size_t WS_U   = WS_H + (size_t)S*D*2;
constexpr size_t WS_O   = WS_U + (size_t)D*F*2;
constexpr size_t WS_KVP = WS_O + (size_t)S*D*2;
constexpr size_t WS_ZP  = WS_KVP + (size_t)16*8*8*2048;
constexpr size_t WS_G   = WS_ZP + (size_t)16*8*2*64*4;
constexpr size_t WS_XCH = WS_G + 8192;
constexpr size_t WS_FLG = WS_XCH + (size_t)256*32768;
constexpr size_t WS_STM = WS_FLG + 65536;
constexpr size_t WS_END = WS_STM + 65536;
constexpr int FLG_KV = 0;
constexpr int FLG_X  = 128;

template<int OFF> DI void ds_tr(u32x2& d, unsigned a){ asm volatile("ds_read_b64_tr_b16 %0, %1 offset:%2" : "=v"(d) : "v"(a), "i"(OFF)); }
template<int OFF> DI void ds_b128(u32x4& d, unsigned a){ asm volatile("ds_read_b128 %0, %1 offset:%2" : "=v"(d) : "v"(a), "i"(OFF)); }
DI void mfma(f32x4& c, const u32x4& a, const u32x4& b){ asm volatile("v_mfma_f32_16x16x32_bf16 %0, %1, %2, %0" : "+v"(c) : "v"(a), "v"(b)); }
DI f32x4 mfma_b(const u32x4& a, const u32x4& b, const f32x4& c){ return __builtin_amdgcn_mfma_f32_16x16x32_bf16(__builtin_bit_cast(bf16x8,a), __builtin_bit_cast(bf16x8,b), c, 0,0,0); }
template<int IMM> DI void dma(unsigned voff, const i32x4& srd, unsigned soff){
  asm volatile("buffer_load_dwordx4 %0, %1, %2 offen offset:%3 lds" :: "v"(voff), "s"(srd), "s"(soff), "i"(IMM) : "memory"); }
template<int V> DI void set_m0(unsigned base){ asm volatile("s_add_u32 m0, %0, %1" :: "s"(base), "i"(V) : "scc"); }
DI void nop1(){ asm volatile("s_nop 0"); }
template<int N> DI void waitv(){ asm volatile("s_waitcnt vmcnt(%0)" :: "n"(N) : "memory"); }
template<int N> DI void waitl(){ asm volatile("s_waitcnt lgkmcnt(%0)" :: "n"(N) : "memory"); }
DI void bar(){ asm volatile("s_barrier" ::: "memory"); }
template<int V> DI void vaddi(unsigned& x){ asm volatile("v_add_u32 %0, %1, %0" : "+v"(x) : "i"(V)); }
template<int V> DI void saddi(unsigned& x){ asm volatile("s_add_u32 %0, %0, %1" : "+s"(x) : "i"(V) : "scc"); }
DI void pin(f32x4& x){ asm volatile("" : "+v"(x)); }
DI u32x4 cat(u32x2 lo,u32x2 hi){ return __builtin_shufflevector(lo,hi,0,1,2,3); }
DI unsigned cvtpk(float lo,float hi){ f32x2 v={lo,hi}; bf16x2_t b=__builtin_convertvector(v,bf16x2_t); return __builtin_bit_cast(unsigned,b); }
DI float bflo(unsigned x){ return __uint_as_float(x<<16); }
DI float bfhi(unsigned x){ return __uint_as_float(x&0xffff0000u); }
DI int otid(){ int t=threadIdx.x; asm volatile("" : "+v"(t)); return t; }
DI unsigned rfl(unsigned x){ return __builtin_amdgcn_readfirstlane(x); }
DI i32x4 make_srd(const void* p, unsigned bytes){
  unsigned long long a=(unsigned long long)p; i32x4 s;
  s[0]=(int)rfl((unsigned)a); s[1]=(int)(rfl((unsigned)(a>>32))&0xffff); s[2]=(int)bytes; s[3]=0x00020000; return s; }
DI __amdgpu_buffer_rsrc_t make_rsrc(const void* p, unsigned bytes){ return __builtin_amdgcn_make_buffer_rsrc((void*)p, 0, bytes, 0x00020000); }
DI void st_sc1(__amdgpu_buffer_rsrc_t r, unsigned off, const u32x4& v){ __builtin_amdgcn_raw_buffer_store_b128(v, r, off, 0, 16); }
DI u32x4 ld_sc1(__amdgpu_buffer_rsrc_t r, unsigned off){ return __builtin_amdgcn_raw_buffer_load_b128(r, off, 0, 16); }
DI float xr16_max(float x){ auto r=__builtin_amdgcn_permlane16_swap(__float_as_uint(x),__float_as_uint(x),false,false); return fmaxf(__uint_as_float(r[0]),__uint_as_float(r[1])); }
DI float xr32_max(float x){ auto r=__builtin_amdgcn_permlane32_swap(__float_as_uint(x),__float_as_uint(x),false,false); return fmaxf(__uint_as_float(r[0]),__uint_as_float(r[1])); }
DI float xr16_min(float x){ auto r=__builtin_amdgcn_permlane16_swap(__float_as_uint(x),__float_as_uint(x),false,false); return fminf(__uint_as_float(r[0]),__uint_as_float(r[1])); }
DI float xr32_min(float x){ auto r=__builtin_amdgcn_permlane32_swap(__float_as_uint(x),__float_as_uint(x),false,false); return fminf(__uint_as_float(r[0]),__uint_as_float(r[1])); }
DI float xr16_add(float x){ auto r=__builtin_amdgcn_permlane16_swap(__float_as_uint(x),__float_as_uint(x),false,false); return __uint_as_float(r[0])+__uint_as_float(r[1]); }
DI float xr32_add(float x){ auto r=__builtin_amdgcn_permlane32_swap(__float_as_uint(x),__float_as_uint(x),false,false); return __uint_as_float(r[0])+__uint_as_float(r[1]); }
DI unsigned flag_ld(const unsigned* p){ return __hip_atomic_load(p, __ATOMIC_RELAXED, __HIP_MEMORY_SCOPE_AGENT); }
DI void flag_st(unsigned* p, unsigned v){ __hip_atomic_store(p, v, __ATOMIC_RELAXED, __HIP_MEMORY_SCOPE_AGENT); }
DI void poll_flags(const unsigned* flags, int first, int n, unsigned epoch, int lane){
  const unsigned* p = flags + (size_t)(first + (lane<n?lane:0))*16;
  for(unsigned spins=0; spins<(1u<<22); ++spins){
    unsigned v=flag_ld(p);
    if(__all(v==epoch)) break;
    __builtin_amdgcn_s_sleep(1);
  }
}
#define STAMP(i)
#define RSTAMP(i)

struct Params {
  const u16* x; const u16* wqkv; const u16* wphi; const u16* wo; const u16* w1; const u16* b1; const u16* w2; const u16* b2; const u16* gw;
  u16* out; char* ws;
};

__global__ __launch_bounds__(512) void k_ln(Params p){
  const int tid=threadIdx.x, w=tid>>6, l=tid&63;
  const int s=blockIdx.x*8+w;
  if(blockIdx.x==0){ unsigned* fl=(unsigned*)(p.ws+WS_FLG); for(int i=tid;i<512*16;i+=512) fl[i]=0; }
  const u32x4 a0=*(const u32x4*)(p.x+(size_t)s*D+l*8), a1=*(const u32x4*)(p.x+(size_t)s*D+512+l*8);
  const u32x4 g0=*(const u32x4*)(p.gw+l*8), g1=*(const u32x4*)(p.gw+512+l*8);
  float v[16], gv[16];
  #pragma unroll
  for(int i=0;i<4;i++){ v[2*i]=bflo(a0[i]); v[2*i+1]=bfhi(a0[i]); v[8+2*i]=bflo(a1[i]); v[8+2*i+1]=bfhi(a1[i]);
                        gv[2*i]=bflo(g0[i]); gv[2*i+1]=bfhi(g0[i]); gv[8+2*i]=bflo(g1[i]); gv[8+2*i+1]=bfhi(g1[i]); }
  float sum=0.f, z=0.f;
  #pragma unroll
  for(int i=0;i<16;i++){ sum+=v[i]; z+=v[i]*gv[i]; }
  #pragma unroll
  for(int o=1;o<64;o<<=1){ sum+=__shfl_xor(sum,o); z+=__shfl_xor(z,o); }
  const float mean=sum*(1.0f/D);
  float q=0.f;
  #pragma unroll
  for(int i=0;i<16;i++){ v[i]-=mean; q+=v[i]*v[i]; }
  #pragma unroll
  for(int o=1;o<64;o<<=1) q+=__shfl_xor(q,o);
  const float r=1.0f/sqrtf(q*(1.0f/D)+1e-5f);
  u32x4 o0,o1;
  #pragma unroll
  for(int i=0;i<4;i++){ o0[i]=cvtpk(v[2*i]*r,v[2*i+1]*r); o1[i]=cvtpk(v[8+2*i]*r,v[8+2*i+1]*r); }
  u16* h=(u16*)(p.ws+WS_H);
  *(u32x4*)(h+(size_t)s*D+l*8)=o0; *(u32x4*)(h+(size_t)s*D+512+l*8)=o1;
  if(l==0) ((float*)(p.ws+WS_G))[s]=1.0f/(1.0f+__expf(-z));
}

namespace qkv {
constexpr int XPART=256*128, WPART=64*384, SLOT=XPART+WPART;
constexpr int WPHI_OFF=2*SLOT;
constexpr int LDS_BYTES=WPHI_OFF+4096;
struct Ctx { unsigned aX[2][2], aW[2][2], vX[4], vW[3]; i32x4 srdX, srdW; unsigned mX, mW, soffX, soffW; };

template<int SET,int RSLOT,int RKS,bool READ,bool DMA,int DSLOT>
DI void step(Ctx& c, f32x4 (&acc)[3][2][4], u32x4 (&fX)[2][4], u32x4 (&fW)[2][3][2]){
  constexpr int NX=SET^1;
  u32x2 wl[3][2], wh[3][2];
  auto RX=[&](auto mc){ constexpr int m=decltype(mc)::value; if constexpr(READ) ds_b128<m*2048>(fX[NX][m], c.aX[RSLOT][RKS]); };
  auto RW=[&](auto sc,auto ec,auto tc){ constexpr int s=decltype(sc)::value,e=decltype(ec)::value,t=decltype(tc)::value;
    if constexpr(READ){ if constexpr(t==0) ds_tr<RKS*12288+s*1024>(wl[s][e], c.aW[RSLOT][e]); else { ds_tr<RKS*12288+s*1024+512>(wh[s][e], c.aW[RSLOT][e]); fW[NX][s][e]=cat(wl[s][e],wh[s][e]); } } };
  auto MX=[&](){ if constexpr(DMA) set_m0<DSLOT*SLOT>(c.mX); };
  auto MW=[&](){ if constexpr(DMA) set_m0<DSLOT*SLOT>(c.mW); };
  auto DX=[&](auto kc){ constexpr int k=decltype(kc)::value; if constexpr(DMA) dma<k*1024>(c.vX[k], c.srdX, c.soffX); };
  auto DW=[&](auto kc){ constexpr int k=decltype(kc)::value; if constexpr(DMA) dma<k*1024>(c.vW[k], c.srdW, c.soffW); };
  auto MM=[&](auto sc,auto ec,auto mc){ constexpr int s=decltype(sc)::value,e=decltype(ec)::value,m=decltype(mc)::value;
    if constexpr(s<2) mfma(acc[s][e][m], fW[SET][s][e], fX[SET][m]); else mfma(acc[s][e][m], fX[SET][m], fW[SET][s][e]); };
  MM(IC(0),IC(0),IC(0)); RX(IC(0)); MW();
  MM(IC(0),IC(0),IC(1)); RW(IC(0),IC(0),IC(0)); DW(IC(0));
  MM(IC(0),IC(0),IC(2)); RW(IC(0),IC(0),IC(1));
  MM(IC(0),IC(0),IC(3)); RX(IC(1)); DW(IC(1));
  MM(IC(0),IC(1),IC(0)); RW(IC(0),IC(1),IC(0));
  MM(IC(0),IC(1),IC(1)); RW(IC(0),IC(1),IC(1)); DW(IC(2));
  MM(IC(0),IC(1),IC(2)); RX(IC(2)); MX();
  MM(IC(0),IC(1),IC(3)); RX(IC(3)); DX(IC(0));
  MM(IC(1),IC(0),IC(0)); RW(IC(1),IC(0),IC(0));
  MM(IC(1),IC(0),IC(1)); RW(IC(1),IC(0),IC(1)); DX(IC(1));
  MM(IC(1),IC(0),IC(2)); RW(IC(1),IC(1),IC(0));
  MM(IC(1),IC(0),IC(3)); RW(IC(1),IC(1),IC(1)); DX(IC(2));
  MM(IC(1),IC(1),IC(0)); RW(IC(2),IC(0),IC(0));
  MM(IC(1),IC(1),IC(1)); RW(IC(2),IC(0),IC(1)); DX(IC(3));
  MM(IC(1),IC(1),IC(2)); RW(IC(2),IC(1),IC(0));
  MM(IC(1),IC(1),IC(3)); RW(IC(2),IC(1),IC(1));
  if constexpr(DMA){ saddi<128>(c.soffX); saddi<64*6144>(c.soffW); }
  MM(IC(2),IC(0),IC(0)); MM(IC(2),IC(0),IC(1)); MM(IC(2),IC(0),IC(2)); MM(IC(2),IC(0),IC(3));
  MM(IC(2),IC(1),IC(0)); MM(IC(2),IC(1),IC(1)); MM(IC(2),IC(1),IC(2)); MM(IC(2),IC(1),IC(3));
  if constexpr(READ) waitl<0>();
}
template<int SL,bool NEXT,bool DMA>
DI void iter(Ctx& c, f32x4 (&acc)[3][2][4], u32x4 (&fX)[2][4], u32x4 (&fW)[2][3][2]){
  step<0,SL,1,true,false,0>(c,acc,fX,fW);
  if constexpr(NEXT){ waitv<0>(); bar(); }
  step<1,SL^1,0,NEXT,DMA,SL>(c,acc,fX,fW);
}

DI void epilogue(const Params& p, char* smem, f32x4 (&acc)[3][2][4], int rt, int ct, int w, unsigned epoch, unsigned long long* stamps);
DI void run(const Params& p, char* smem, int rt, int ct, unsigned epoch, unsigned long long* stamps){
  const unsigned lds0=(unsigned)(size_t)(LAS char*)smem;
  const int tid=threadIdx.x, w=rfl(tid>>6), l=tid&63;
  const int g=l>>4,q=(l&15)>>2,pp=l&3,i16=l&15;
  const int wr=w&3, wh=w>>2;
  Ctx c; f32x4 acc[3][2][4]; u32x4 fX[2][4], fW[2][3][2];
  c.srdX=make_srd(p.x,S*D*2); c.srdW=make_srd(p.wqkv,D*3*D*2);
  c.mX=rfl(lds0+w*4096); c.mW=rfl(lds0+XPART+w*3072);
  #pragma unroll
  for(int k=0;k<4;k++){ const int row=32*w+8*k+(l>>3); const int ch=(l&7)^((row>>1)&7); c.vX[k]=(rt*256+row)*2048+ch*16-1024*k; }
  #pragma unroll
  for(int s=0;s<3;s++){ const int row=8*w+(l>>3); const int sw=((row>>1)&1)|(((row>>3)&1)<<1); const int pos=l&7; const int ch=(((pos>>1)^sw)<<1)|(pos&1);
    c.vW[s]=row*6144+s*2048+ct*128+ch*16-1024*s; }
  #pragma unroll
  for(int sl=0;sl<2;sl++){
    #pragma unroll
    for(int ks=0;ks<2;ks++) c.aX[sl][ks]=lds0+sl*SLOT+(64*wr+i16)*128+(((4*ks+g)^((i16>>1)&7))*16);
    const int sw2=((q>>1)&1)|((g&1)<<1);
    #pragma unroll
    for(int e=0;e<2;e++) c.aW[sl][e]=lds0+sl*SLOT+XPART+g*3072+q*128+(((2*wh+e)^sw2)*32)+8*pp;
  }
  #pragma unroll
  for(int s=0;s<3;s++)
  #pragma unroll
  for(int e=0;e<2;e++)
  #pragma unroll
  for(int m=0;m<4;m++){ acc[s][e][m]=(f32x4){0,0,0,0}; pin(acc[s][e][m]); }
  STAMP(1);
  c.soffX=0; c.soffW=0;
  if(w<4){ i32x4 srdP=make_srd(p.wphi,32*32*32*2); set_m0<0>(rfl(lds0+WPHI_OFF+w*1024)); nop1(); dma<0>((unsigned)(ct*4096+w*1024+l*16), srdP, 0u); }
  auto tile=[&](auto tc){ constexpr int T=decltype(tc)::value;
    set_m0<T*SLOT>(c.mX); nop1(); dma<0>(c.vX[0],c.srdX,c.soffX); dma<1024>(c.vX[1],c.srdX,c.soffX); dma<2048>(c.vX[2],c.srdX,c.soffX); dma<3072>(c.vX[3],c.srdX,c.soffX);
    set_m0<T*SLOT>(c.mW); nop1(); dma<0>(c.vW[0],c.srdW,c.soffW); dma<1024>(c.vW[1],c.srdW,c.soffW); dma<2048>(c.vW[2],c.srdW,c.soffW);
    saddi<128>(c.soffX); saddi<64*6144>(c.soffW); };
  tile(IC(0)); tile(IC(1));
  waitv<7>(); bar();
  {
    ds_b128<0>(fX[0][0],c.aX[0][0]); ds_b128<2048>(fX[0][1],c.aX[0][0]); ds_b128<4096>(fX[0][2],c.aX[0][0]); ds_b128<6144>(fX[0][3],c.aX[0][0]);
    auto rd=[&](auto sc,auto ec){ constexpr int s=decltype(sc)::value,e=decltype(ec)::value; u32x2 lo,hi; ds_tr<s*1024>(lo,c.aW[0][e]); ds_tr<s*1024+512>(hi,c.aW[0][e]); fW[0][s][e]=cat(lo,hi); };
    rd(IC(0),IC(0)); rd(IC(0),IC(1)); rd(IC(1),IC(0)); rd(IC(1),IC(1)); rd(IC(2),IC(0)); rd(IC(2),IC(1));
    waitl<0>(); }
  STAMP(2);
  for(int t=0;t<14;t+=2){ iter<0,true,true>(c,acc,fX,fW); iter<1,true,true>(c,acc,fX,fW); }
  iter<0,true,false>(c,acc,fX,fW); iter<1,false,false>(c,acc,fX,fW);
  asm volatile("s_nop 7\n\ts_nop 7" ::: "memory");
  #pragma unroll
  for(int s=0;s<3;s++)
  #pragma unroll
  for(int e=0;e<2;e++)
  #pragma unroll
  for(int m=0;m<4;m++) pin(acc[s][e][m]);
  STAMP(3);
  epilogue(p,smem,acc,rt,ct,w,epoch,stamps);
}
DI void epilogue(const Params& p, char* smem, f32x4 (&acc)[3][2][4], int rt, int ct, int w, unsigned epoch, unsigned long long* stamps){
  const unsigned lds0=(unsigned)(size_t)(LAS char*)smem;
  LAS char* sm=(LAS char*)smem;
  const int tid=otid(), l=tid&63;
  const int g=l>>4,q=(l&15)>>2,pp=l&3,i16=l&15;
  const int wr=w&3, wh=w>>2;
  constexpr int TPHI=0, VREG=65536, SHR=98304;
  u32x4 Ap[2];
  { const unsigned b=lds0+WPHI_OFF+wh*2048+(4*g+q)*64+pp*8;
    u32x2 l0,h0,l1,h1; ds_tr<0>(l0,b); ds_tr<1024>(h0,b); ds_tr<32>(l1,b); ds_tr<32+1024>(h1,b); waitl<0>();
    asm volatile("" : "+v"(l0),"+v"(h0),"+v"(l1),"+v"(h1));
    Ap[0]=cat(l0,h0); Ap[1]=cat(l1,h1); }
  constexpr float L2E=1.4426950408889634f;
  u32x4 phq[4][2];
  const int swp=(((i16>>1)&3)<<2)|(((i16>>3)&1)<<1)|(i16&1);
  #pragma unroll
  for(int X=0;X<2;X++){
    #pragma unroll
    for(int m=0;m<4;m++){
      u32x4 B; B[0]=cvtpk(acc[X][0][m][0],acc[X][0][m][1]); B[1]=cvtpk(acc[X][0][m][2],acc[X][0][m][3]); B[2]=cvtpk(acc[X][1][m][0],acc[X][1][m][1]); B[3]=cvtpk(acc[X][1][m][2],acc[X][1][m][3]);
      const f32x4 zero={0,0,0,0};
      f32x4 P0=mfma_b(Ap[0],B,zero), P1=mfma_b(Ap[1],B,zero);
      float mx=fmaxf(fmaxf(fmaxf(P0[0],P0[1]),fmaxf(P0[2],P0[3])),fmaxf(fmaxf(P1[0],P1[1]),fmaxf(P1[2],P1[3])));
      float mn=fminf(fminf(fminf(P0[0],P0[1]),fminf(P0[2],P0[3])),fminf(fminf(P1[0],P1[1]),fminf(P1[2],P1[3])));
      mx=xr32_max(xr16_max(mx)); mn=xr32_min(xr16_min(mn));
      const float mxl=mx*L2E, mnl=mn*L2E;
      float ep[8], en[8];
      #pragma unroll
      for(int i=0;i<4;i++){ ep[i]=__builtin_amdgcn_exp2f(P0[i]*L2E-mxl); ep[4+i]=__builtin_amdgcn_exp2f(P1[i]*L2E-mxl); en[i]=__builtin_amdgcn_exp2f(mnl-P0[i]*L2E); en[4+i]=__builtin_amdgcn_exp2f(mnl-P1[i]*L2E); }
      float sp=((ep[0]+ep[1])+(ep[2]+ep[3]))+((ep[4]+ep[5])+(ep[6]+ep[7])), sn=((en[0]+en[1])+(en[2]+en[3]))+((en[4]+en[5])+(en[6]+en[7]));
      sp=xr32_add(xr16_add(sp)); sn=xr32_add(xr16_add(sn));
      const float rp=__builtin_amdgcn_rcpf(sp), rn=__builtin_amdgcn_rcpf(sn);
      u32x4 hp,hn;
      hp[0]=cvtpk(ep[0]*rp,ep[1]*rp); hp[1]=cvtpk(ep[2]*rp,ep[3]*rp); hp[2]=cvtpk(ep[4]*rp,ep[5]*rp); hp[3]=cvtpk(ep[6]*rp,ep[7]*rp);
      hn[0]=cvtpk(en[0]*rn,en[1]*rn); hn[1]=cvtpk(en[2]*rn,en[3]*rn); hn[2]=cvtpk(en[4]*rn,en[5]*rn); hn[3]=cvtpk(en[6]*rn,en[7]*rn);
      if(X==0){ phq[m][0]=hp; phq[m][1]=hn; }
      else {
        LAS char* row=sm+TPHI+w*8192+(16*m+i16)*128;
        *(LAS u32x2*)(row+(((0+g)^swp)*8))=(u32x2){hp[0],hp[1]};
        *(LAS u32x2*)(row+(((4+g)^swp)*8))=(u32x2){hp[2],hp[3]};
        *(LAS u32x2*)(row+(((8+g)^swp)*8))=(u32x2){hn[0],hn[1]};
        *(LAS u32x2*)(row+(((12+g)^swp)*8))=(u32x2){hn[2],hn[3]};
      }
    }
  }
  #pragma unroll
  for(int mp=0;mp<2;mp++)
  #pragma unroll
  for(int e=0;e<2;e++){
    u32x4 fv; fv[0]=cvtpk(acc[2][e][2*mp][0],acc[2][e][2*mp][1]); fv[1]=cvtpk(acc[2][e][2*mp][2],acc[2][e][2*mp][3]); fv[2]=cvtpk(acc[2][e][2*mp+1][0],acc[2][e][2*mp+1][1]); fv[3]=cvtpk(acc[2][e][2*mp+1][2],acc[2][e][2*mp+1][3]);
    *(LAS u32x4*)(sm+VREG+w*4096+(mp*2+e)*1024+l*16)=fv;
  }
  __syncthreads();
  STAMP(4);
  f32x4 kv0={0,0,0,0},kv1={0,0,0,0},zz={0,0,0,0};
  { const u32x4 ones={0x3F803F80u,0x3F803F80u,0x3F803F80u,0x3F803F80u};
    const int r15=4*g+q; const int swr=(((r15>>1)&3)<<2)|(((r15>>3)&1)<<1)|(r15&1);
    const unsigned ab=lds0+TPHI+wh*4*8192+r15*128+(((4*wr+pp)^swr)*8);
    #pragma unroll
    for(int sw_=0;sw_<4;sw_++){
      u32x2 a0,a1,a2,a3;
      if(sw_==0){ ds_tr<0*8192+0>(a0,ab); ds_tr<0*8192+2048>(a1,ab); ds_tr<0*8192+4096>(a2,ab); ds_tr<0*8192+6144>(a3,ab); }
      if(sw_==1){ ds_tr<1*8192+0>(a0,ab); ds_tr<1*8192+2048>(a1,ab); ds_tr<1*8192+4096>(a2,ab); ds_tr<1*8192+6144>(a3,ab); }
      if(sw_==2){ ds_tr<2*8192+0>(a0,ab); ds_tr<2*8192+2048>(a1,ab); ds_tr<2*8192+4096>(a2,ab); ds_tr<2*8192+6144>(a3,ab); }
      if(sw_==3){ ds_tr<3*8192+0>(a0,ab); ds_tr<3*8192+2048>(a1,ab); ds_tr<3*8192+4096>(a2,ab); ds_tr<3*8192+6144>(a3,ab); }
      LAS char* vb=sm+VREG+(wh*4+sw_)*4096+l*16;
      u32x4 v00=*(LAS u32x4*)(vb), v01=*(LAS u32x4*)(vb+1024), v10=*(LAS u32x4*)(vb+2048), v11=*(LAS u32x4*)(vb+3072);
      asm volatile("s_waitcnt lgkmcnt(0)" : "+v"(a0),"+v"(a1),"+v"(a2),"+v"(a3) :: "memory");
      const u32x4 A0=cat(a0,a1), A1=cat(a2,a3);
      kv0=mfma_b(A0,v00,kv0); kv1=mfma_b(A0,v01,kv1); zz=mfma_b(A0,ones,zz);
      kv0=mfma_b(A1,v10,kv0); kv1=mfma_b(A1,v11,kv1); zz=mfma_b(A1,ones,zz);
    }
  }
  char* ws=p.ws;
  const __amdgpu_buffer_rsrc_t rKV=make_rsrc(ws+WS_KVP,16*8*8*2048), rZ=make_rsrc(ws+WS_ZP,16*8*2*64*4);
  st_sc1(rKV,(unsigned)(((ct*8+rt)*8+w)*2048+l*16),__builtin_bit_cast(u32x4,kv0));
  st_sc1(rKV,(unsigned)(((ct*8+rt)*8+w)*2048+1024+l*16),__builtin_bit_cast(u32x4,kv1));
  if(i16==0) st_sc1(rZ,(unsigned)((((ct*8+rt)*2+wh)*64+16*wr+4*g)*4),__builtin_bit_cast(u32x4,zz));
  asm volatile("s_waitcnt vmcnt(0)" ::: "memory");
  __syncthreads();
  unsigned* flags=(unsigned*)(ws+WS_FLG);
  if(tid==0) flag_st(flags+(FLG_KV+ct*8+rt)*16, epoch);
  if(w==0) poll_flags(flags, FLG_KV+ct*8, 8, epoch, l);
  __syncthreads();
  STAMP(5);
  { f32x4 s0={0,0,0,0},s1={0,0,0,0},sz={0,0,0,0};
    u32x4 t0[8],t1[8],tz[8];
    #pragma unroll
    for(int r=0;r<8;r++){ t0[r]=ld_sc1(rKV,(unsigned)(((ct*8+r)*8+w)*2048+l*16)); t1[r]=ld_sc1(rKV,(unsigned)(((ct*8+r)*8+w)*2048+1024+l*16)); tz[r]=ld_sc1(rZ,(unsigned)((((ct*8+r)*2+wh)*64+16*wr+4*g)*4)); }
    #pragma unroll
    for(int r=0;r<8;r++){ s0+=__builtin_bit_cast(f32x4,t0[r]); s1+=__builtin_bit_cast(f32x4,t1[r]); sz+=__builtin_bit_cast(f32x4,tz[r]); }
    LAS char* sb=sm+SHR+w*1536+l*8;
    *(LAS u32x2*)(sb)=(u32x2){cvtpk(s0[0],s0[1]),cvtpk(s0[2],s0[3])};
    *(LAS u32x2*)(sb+512)=(u32x2){cvtpk(s1[0],s1[1]),cvtpk(s1[2],s1[3])};
    *(LAS u32x2*)(sb+1024)=(u32x2){cvtpk(sz[0],sz[1]),cvtpk(sz[2],sz[3])};
  }
  __syncthreads();
  { u32x4 kf[2][2], zf[2];
    #pragma unroll
    for(int sg=0;sg<2;sg++){
      LAS char* b0=sm+SHR+(wh*4+2*sg)*1536+l*8; LAS char* b1=b0+1536;
      kf[sg][0]=cat(*(LAS u32x2*)(b0),*(LAS u32x2*)(b1)); kf[sg][1]=cat(*(LAS u32x2*)(b0+512),*(LAS u32x2*)(b1+512)); zf[sg]=cat(*(LAS u32x2*)(b0+1024),*(LAS u32x2*)(b1+1024)); }
    const __amdgpu_buffer_rsrc_t rO=make_rsrc(ws+WS_O,S*D*2);
    const int offg=(g&1)*16+(g>>1)*8;
    #pragma unroll
    for(int m=0;m<4;m++){
      const f32x4 zero={0,0,0,0};
      f32x4 n0=mfma_b(kf[0][0],phq[m][0],zero); n0=mfma_b(kf[1][0],phq[m][1],n0);
      f32x4 n1=mfma_b(kf[0][1],phq[m][0],zero); n1=mfma_b(kf[1][1],phq[m][1],n1);
      f32x4 dn=mfma_b(zf[0],phq[m][0],zero); dn=mfma_b(zf[1],phq[m][1],dn);
      const float rd=1.0f/(dn[0]+1e-6f);
      unsigned a0=cvtpk(n0[0]*rd,n0[1]*rd), a1=cvtpk(n0[2]*rd,n0[3]*rd), b0=cvtpk(n1[0]*rd,n1[1]*rd), b1=cvtpk(n1[2]*rd,n1[3]*rd);
      auto r0=__builtin_amdgcn_permlane16_swap(a0,b0,false,false); auto r1=__builtin_amdgcn_permlane16_swap(a1,b1,false,false);
      u32x4 ov={r0[0],r1[0],r0[1],r1[1]};
      const int tok=rt*256+64*wr+16*m+i16;
      st_sc1(rO,(unsigned)(tok*2048+(ct*2+wh)*64+offg*2),ov);
    }
  }
  STAMP(6);
}
}

namespace mix1 {
constexpr int HPART=64*256, WPART=64*448, SLOT=HPART+WPART+4096;
constexpr int BIAS_OFF=3*SLOT;
constexpr int LDS_BYTES=BIAS_OFF+1024;
struct Ctx { unsigned bH[4], bW[2], vH[2], vW[4]; i32x4 srdH, srdW; unsigned mH, mW, soffH, soffW; };
template<int SET,int SL,bool READ,bool DMA,int VM>
DI void iter(Ctx& c, f32x4 (&acc)[7][4], u32x4 (&fH)[2][4], u32x4 (&fW)[2][7]){
  constexpr int NX=SET^1;
  constexpr int RD=((SL+1)%3==2)? -2*SLOT : SLOT;
  if constexpr(VM>=0){ waitv<VM>(); bar(); }
  u32x2 hl[4],hh[4],wl[7],wh[7];
  auto RH=[&](auto ic,auto hc){ constexpr int i=decltype(ic)::value,h=decltype(hc)::value;
    if constexpr(READ){ if constexpr(h==0) ds_tr<0>(hl[i],c.bH[i]); else { ds_tr<1024>(hh[i],c.bH[i]); fH[NX][i]=cat(hl[i],hh[i]); vaddi<RD>(c.bH[i]); } } };
  auto RW=[&](auto ic,auto hc){ constexpr int i=decltype(ic)::value,h=decltype(hc)::value; constexpr int off=HPART+(i&~1)*32;
    if constexpr(READ){ if constexpr(h==0) ds_tr<off>(wl[i],c.bW[i&1]); else { ds_tr<off+4*448>(wh[i],c.bW[i&1]); fW[NX][i]=cat(wl[i],wh[i]); if constexpr(i>=5) vaddi<RD>(c.bW[i&1]); } } };
  auto MH=[&](){ if constexpr(DMA) set_m0<SL*SLOT>(c.mH); };
  auto MW=[&](){ if constexpr(DMA) set_m0<SL*SLOT>(c.mW); };
  auto DH=[&](auto kc){ constexpr int k=decltype(kc)::value; if constexpr(DMA) dma<k*1024>(c.vH[k],c.srdH,c.soffH); };
  auto DW=[&](auto kc){ constexpr int k=decltype(kc)::value; if constexpr(DMA) dma<k*1024>(c.vW[k],c.srdW,c.soffW); };
  auto MM=[&](auto ac,auto bc){ constexpr int a=decltype(ac)::value,b=decltype(bc)::value; mfma(acc[a][b],fW[SET][a],fH[SET][b]); };
  MM(IC(0),IC(0)); RH(IC(0),IC(0)); MW();
  MM(IC(0),IC(1)); RH(IC(0),IC(1));
  MM(IC(0),IC(2)); RW(IC(0),IC(0)); DW(IC(0));
  MM(IC(0),IC(3)); RW(IC(0),IC(1));
  MM(IC(1),IC(0)); RH(IC(1),IC(0));
  MM(IC(1),IC(1)); RH(IC(1),IC(1)); DW(IC(1));
  MM(IC(1),IC(2)); RW(IC(1),IC(0));
  MM(IC(1),IC(3)); RW(IC(1),IC(1));
  MM(IC(2),IC(0)); RH(IC(2),IC(0));
  MM(IC(2),IC(1)); RH(IC(2),IC(1)); DW(IC(2));
  MM(IC(2),IC(2)); RW(IC(2),IC(0));
  MM(IC(2),IC(3)); RW(IC(2),IC(1));
  MM(IC(3),IC(0)); RH(IC(3),IC(0));
  MM(IC(3),IC(1)); RH(IC(3),IC(1)); DW(IC(3));
  MM(IC(3),IC(2)); RW(IC(3),IC(0)); MH();
  MM(IC(3),IC(3)); RW(IC(3),IC(1));
  MM(IC(4),IC(0)); RW(IC(4),IC(0)); DH(IC(0));
  MM(IC(4),IC(1)); RW(IC(4),IC(1));
  MM(IC(4),IC(2)); RW(IC(5),IC(0));
  MM(IC(4),IC(3)); RW(IC(5),IC(1)); DH(IC(1));
  MM(IC(5),IC(0)); RW(IC(6),IC(0));
  MM(IC(5),IC(1)); RW(IC(6),IC(1));
  if constexpr(DMA){ saddi<64*2048>(c.soffH); saddi<64*7168>(c.soffW); }
  MM(IC(5),IC(2)); MM(IC(5),IC(3));
  MM(IC(6),IC(0)); MM(IC(6),IC(1)); MM(IC(6),IC(2)); MM(IC(6),IC(3));
  if constexpr(READ) waitl<0>();
}
DI float gelu1(float x){
  const float k1=-2.0f*0.7978845608028654f*1.4426950408889634f, k2=k1*0.044715f;
  const float t=x*__builtin_fmaf(x*x,k2,k1);
  return x*__builtin_amdgcn_rcpf(1.0f+__builtin_amdgcn_exp2f(t));
}
template<int KG>
DI void epilogue(const Params& p, LAS char* sm, f32x4 (&acc)[7][4], int w, int d0, int f0){
  const int tid=otid(), l=tid&63, dh=w&1, fh=(w>>1)&1;
  const int g=l>>4, i16=l&15;
  { LAS char* wb=sm+w*14336+l*16;
    #pragma unroll
    for(int a=0;a<7;a++){ *(LAS f32x4*)(wb+(a*2+0)*1024)=acc[a][2*(1-KG)]; *(LAS f32x4*)(wb+(a*2+1)*1024)=acc[a][2*(1-KG)+1]; } }
  u32x2 bb[7];
  #pragma unroll
  for(int a=0;a<7;a++) bb[a]=*(LAS u32x2*)(sm+BIAS_OFF+((fh*7+a)*16+4*g)*2);
  __syncthreads();
  u32x2 res[7][2];
  { LAS char* rb=sm+(w^4)*14336+l*16;
    #pragma unroll
    for(int a=0;a<7;a++){
      const float b0=bflo(bb[a][0]),b1=bfhi(bb[a][0]),b2=bflo(bb[a][1]),b3=bfhi(bb[a][1]);
      #pragma unroll
      for(int b=0;b<2;b++){
        const f32x4 x=acc[a][2*KG+b]+*(LAS f32x4*)(rb+(a*2+b)*1024);
        res[a][b][0]=cvtpk(gelu1(x[0]+b0),gelu1(x[1]+b1)); res[a][b][1]=cvtpk(gelu1(x[2]+b2),gelu1(x[3]+b3));
      } } }
  __syncthreads();
  constexpr int RSO=464;
  #pragma unroll
  for(int a=0;a<7;a++)
  #pragma unroll
  for(int b=0;b<2;b++) *(LAS u32x2*)(sm+(64*dh+16*(2*KG+b)+i16)*RSO+((fh*7+a)*16+4*g)*2)=res[a][b];
  __syncthreads();
  const __amdgpu_buffer_rsrc_t rU=make_rsrc(p.ws+WS_U,D*F*2);
  #pragma unroll
  for(int k=0;k<7;k++){ const int n=k*512+tid; const int row=n/28, ch=n-row*28; const u32x4 x=*(LAS u32x4*)(sm+row*RSO+ch*16); st_sc1(rU,(unsigned)((d0+row)*7168+f0*2+ch*16),x); }
}
DI void run(const Params& p, char* smem, int dt, int ft, unsigned long long* stamps){
  const unsigned lds0=(unsigned)(size_t)(LAS char*)smem;
  const int tid=threadIdx.x, w=rfl(tid>>6), l=tid&63;
  const int g=l>>4,q=(l&15)>>2,pp=l&3;
  const int dh=w&1, fh=(w>>1)&1, kg=w>>2;
  const int d0=dt*128, f0=ft*224;
  Ctx c; f32x4 acc[7][4]; u32x4 fH[2][4], fW[2][7];
  c.srdH=make_srd(p.ws+WS_H,S*D*2); c.srdW=make_srd(p.w1,S*F*2);
  c.mH=rfl(lds0+w*2048); c.mW=rfl(lds0+HPART+w*4096);
  #pragma unroll
  for(int k=0;k<2;k++){ const int row=8*w+4*k+(l>>4); const int sw=(row&3)|(((row>>3)&1)<<2); const int pos=l&15; const int ch=(((pos>>1)^sw)<<1)|(pos&1);
    c.vH[k]=row*2048+d0*2+ch*16-1024*k; }
  #pragma unroll
  for(int k=0;k<4;k++){ const int n=64*(4*w+k)+l; const int row=n/28,pos=n-row*28; const int ch=pos^(((row>>3)&1)<<1); c.vW[k]=(w<7)? (unsigned)(row*7168+f0*2+ch*16-1024*k) : 0x80000000u; }
  { const int row=32*kg+8*g+q; const int sw=q|((g&1)<<2);
    #pragma unroll
    for(int e=0;e<4;e++) c.bH[e]=lds0+row*256+(((4*dh+e)^sw)*32)+8*pp;
    const int b3=g&1;
    #pragma unroll
    for(int e=0;e<2;e++) c.bW[e]=lds0+row*448+(((fh*7+e)^b3)*32)+8*pp; }
  #pragma unroll
  for(int a=0;a<7;a++)
  #pragma unroll
  for(int b=0;b<4;b++){ acc[a][b]=(f32x4){0,0,0,0}; pin(acc[a][b]); }
  STAMP(1);
  c.soffH=0; c.soffW=0;
  if(w==7){ i32x4 srdB=make_srd(p.b1,F*2); set_m0<0>(rfl(lds0+BIAS_OFF)); nop1(); dma<0>((unsigned)(f0*2+l*16), srdB, 0u); }
  auto tile=[&](auto tc){ constexpr int T=decltype(tc)::value;
    set_m0<T*SLOT>(c.mW); nop1(); dma<0>(c.vW[0],c.srdW,c.soffW); dma<1024>(c.vW[1],c.srdW,c.soffW); dma<2048>(c.vW[2],c.srdW,c.soffW); dma<3072>(c.vW[3],c.srdW,c.soffW);
    set_m0<T*SLOT>(c.mH); nop1(); dma<0>(c.vH[0],c.srdH,c.soffH); dma<1024>(c.vH[1],c.srdH,c.soffH);
    saddi<64*2048>(c.soffH); saddi<64*7168>(c.soffW); };
  tile(IC(0)); tile(IC(1)); tile(IC(2));
  waitv<12>();
  bar();
  { auto rd2=[&](auto o0,auto o1,u32x4& f,unsigned a){ u32x2 lo,hi; ds_tr<decltype(o0)::value>(lo,a); ds_tr<decltype(o1)::value>(hi,a); f=cat(lo,hi); };
    rd2(IC(0),IC(1024),fH[0][0],c.bH[0]); rd2(IC(0),IC(1024),fH[0][1],c.bH[1]); rd2(IC(0),IC(1024),fH[0][2],c.bH[2]); rd2(IC(0),IC(1024),fH[0][3],c.bH[3]);
    rd2(IC(HPART),IC(HPART+4*448),fW[0][0],c.bW[0]); rd2(IC(HPART),IC(HPART+4*448),fW[0][1],c.bW[1]);
    rd2(IC(HPART+64),IC(HPART+64+4*448),fW[0][2],c.bW[0]); rd2(IC(HPART+64),IC(HPART+64+4*448),fW[0][3],c.bW[1]);
    rd2(IC(HPART+128),IC(HPART+128+4*448),fW[0][4],c.bW[0]); rd2(IC(HPART+128),IC(HPART+128+4*448),fW[0][5],c.bW[1]);
    rd2(IC(HPART+192),IC(HPART+192+4*448),fW[0][6],c.bW[0]);
    vaddi<SLOT>(c.bH[0]); vaddi<SLOT>(c.bH[1]); vaddi<SLOT>(c.bH[2]); vaddi<SLOT>(c.bH[3]); vaddi<SLOT>(c.bW[0]); vaddi<SLOT>(c.bW[1]);
    waitl<0>(); }
  STAMP(2);
  { constexpr int ND=6;
    for(int t=0;t<24;t+=6){
      iter<0,0,true,true,ND>(c,acc,fH,fW); iter<1,1,true,true,ND>(c,acc,fH,fW); iter<0,2,true,true,ND>(c,acc,fH,fW);
      iter<1,0,true,true,ND>(c,acc,fH,fW); iter<0,1,true,true,ND>(c,acc,fH,fW); iter<1,2,true,true,ND>(c,acc,fH,fW); }
    iter<0,0,true,true,ND>(c,acc,fH,fW); iter<1,1,true,true,ND>(c,acc,fH,fW); iter<0,2,true,true,ND>(c,acc,fH,fW);
    iter<1,0,true,true,ND>(c,acc,fH,fW); iter<0,1,true,true,ND>(c,acc,fH,fW);
    iter<1,2,true,false,ND>(c,acc,fH,fW);
    iter<0,0,true,false,0>(c,acc,fH,fW);
    iter<1,1,false,false,-1>(c,acc,fH,fW); }
  asm volatile("s_nop 7\n\ts_nop 7" ::: "memory");
  #pragma unroll
  for(int a=0;a<7;a++)
  #pragma unroll
  for(int b=0;b<4;b++) pin(acc[a][b]);
  STAMP(3);
  bar();
  if(kg==0) epilogue<0>(p,(LAS char*)smem,acc,w,d0,f0); else epilogue<1>(p,(LAS char*)smem,acc,w,d0,f0);
  STAMP(4);
}
}

__global__ __launch_bounds__(512,2) void k_phaseA(Params p){
  extern __shared__ __attribute__((aligned(16))) char smem[];
  unsigned long long* stamps=(unsigned long long*)(p.ws+WS_STM);
  (void)stamps;
  RSTAMP(0);
  const int b=blockIdx.x, xcd=b&7, j=b>>3;
  if(xcd<4) qkv::run(p,smem,j&7,xcd*4+(j>>3),1u,stamps);
  else mix1::run(p,smem,j&7,(xcd-4)*4+(j>>3),stamps);
  RSTAMP(15);
}

namespace fin {
constexpr int PART=16384, SLOT=2*PART, NS=4, GB_OFF=NS*SLOT, LDS_BYTES=GB_OFF+2048;
struct Src { i32x4 srd; unsigned v[2]; unsigned soff; };
struct Bases;
template<int SET,int SL,bool READ,bool RPA,int DMAK  ,int VM,class BT>
DI void iter(const BT& RB, unsigned mS, unsigned mD, Src& sS, Src& sD, f32x4 (&acc)[4][4], u32x4 (&fS)[2][4], u32x4 (&fD)[2][4]){
  constexpr int NX=SET^1, RS=(SL+1)&3, DS=(SL+3)&3;
  constexpr int RHI=RS>>1, RO=(RS&1)*SLOT;
  if constexpr(VM>=0){ waitv<VM>(); bar(); }
  u32x2 tl[4],th[4];
  auto RR=[&](auto ic){ constexpr int i=decltype(ic)::value;
    if constexpr(READ){ if constexpr(RPA) ds_b128<RO+i*2048>(fS[NX][i],RB.aR[RHI]); else ds_b128<RO+PART+i*2048>(fD[NX][i],RB.aR[RHI]); } };
  auto RT=[&](auto ic,auto hc){ constexpr int i=decltype(ic)::value,h=decltype(hc)::value;
    if constexpr(READ){
      if constexpr(RPA){ if constexpr(h==0) ds_tr<RO+PART>(tl[i],RB.aT[RHI][i]); else { ds_tr<RO+PART+1024>(th[i],RB.aT[RHI][i]); fD[NX][i]=cat(tl[i],th[i]); } }
      else { if constexpr(h==0) ds_tr<RO>(tl[i],RB.aT[RHI][i]); else { ds_tr<RO+1024>(th[i],RB.aT[RHI][i]); fS[NX][i]=cat(tl[i],th[i]); } } } };
  auto MS=[&](){ if constexpr(DMAK!=0) set_m0<DS*SLOT>(mS); };
  auto MD=[&](){ if constexpr(DMAK!=0) set_m0<DS*SLOT>(mD); };
  auto DSs=[&](auto kc){ constexpr int k=decltype(kc)::value; if constexpr(DMAK!=0) dma<k*1024>(sS.v[k],sS.srd,sS.soff); };
  auto DDd=[&](auto kc){ constexpr int k=decltype(kc)::value; if constexpr(DMAK!=0) dma<k*1024>(sD.v[k],sD.srd,sD.soff); };
  auto MM=[&](auto ac,auto bc){ constexpr int a=decltype(ac)::value,b=decltype(bc)::value; mfma(acc[a][b],fD[SET][a],fS[SET][b]); };
  MM(IC(0),IC(0)); RR(IC(0)); MS();
  MM(IC(0),IC(1)); RT(IC(0),IC(0)); DSs(IC(0));
  MM(IC(0),IC(2)); RT(IC(0),IC(1));
  MM(IC(0),IC(3)); RR(IC(1)); DSs(IC(1));
  MM(IC(1),IC(0)); RT(IC(1),IC(0)); MD();
  MM(IC(1),IC(1)); RT(IC(1),IC(1)); DDd(IC(0));
  MM(IC(1),IC(2)); RR(IC(2));
  MM(IC(1),IC(3)); RT(IC(2),IC(0)); DDd(IC(1));
  MM(IC(2),IC(0)); RT(IC(2),IC(1));
  MM(IC(2),IC(1)); RR(IC(3));
  if constexpr(DMAK==1){ saddi<128>(sS.soff); saddi<64*2048>(sD.soff); }
  if constexpr(DMAK==2){ saddi<64*4096>(sS.soff); saddi<128>(sD.soff); }
  MM(IC(2),IC(2)); RT(IC(3),IC(0));
  MM(IC(2),IC(3)); RT(IC(3),IC(1));
  MM(IC(3),IC(0)); MM(IC(3),IC(1)); MM(IC(3),IC(2)); MM(IC(3),IC(3));
  if constexpr(READ) waitl<0>();
}
struct Bases { unsigned aR[2], aT[2][4]; };
template<bool PA> DI void mk_bases(Bases& B, unsigned lds0, int wsd, int wd, int kg){
  const int l=otid()&63, g=l>>4,q=(l&15)>>2,pp=l&3,i16=l&15;
  #pragma unroll
  for(int hi=0;hi<2;hi++){
    const unsigned base=lds0+hi*2*SLOT;
    B.aR[hi]=base+(64*(PA?wsd:wd)+i16)*128+(((4*kg+g)^((i16>>1)&7))*16);
    const int row=32*kg+8*g+q; const int sw=q|((g&1)<<2);
    #pragma unroll
    for(int e=0;e<4;e++) B.aT[hi][e]=base+row*256+(((4*(PA?wd:wsd)+e)^sw)*32)+8*pp;
  }
}
template<bool PA> DI void mk_src(const Params& p, Src& sS, Src& sD, int w, int s0, int d0, int half){
  const int l=otid()&63;
  #pragma unroll
  for(int k=0;k<2;k++){
    const int rowR=16*w+8*k+(l>>3); const int chR=(l&7)^((rowR>>1)&7);
    const int rowT=8*w+4*k+(l>>4); const int sw=(rowT&3)|(((rowT>>3)&1)<<2); const int pos=l&15; const int chT=(((pos>>1)^sw)<<1)|(pos&1);
    if constexpr(PA){ sS.v[k]=(s0+rowR)*2048+chR*16-1024*k; sD.v[k]=rowT*2048+d0*2+chT*16-1024*k; }
    else            { sS.v[k]=rowT*4096+s0*2+chT*16-1024*k; sD.v[k]=(d0+rowR)*7168+chR*16-1024*k; }
  }
  if constexpr(PA){ sS.srd=make_srd(p.ws+WS_O,S*D*2); sD.srd=make_srd(p.wo,D*D*2); sS.soff=0; sD.soff=0; }
  else { sS.srd=make_srd(p.w2,F*S*2); sD.srd=make_srd(p.ws+WS_U,D*F*2); sS.soff=rfl(half? 20*64*4096 : 0); sD.soff=rfl(half? 20*128 : 0); }
}
DI void run(const Params& p, char* smem, int st, int dt, int half, unsigned epoch, unsigned long long* stamps){
  const unsigned lds0=(unsigned)(size_t)(LAS char*)smem;
  LAS char* sm=(LAS char*)smem;
  const int w=rfl(threadIdx.x>>6);
  const int wsd=w&1, wd=(w>>1)&1, kg=w>>2;
  const int s0=st*128, d0=dt*128;
  const unsigned mS=rfl(lds0+w*2048), mD=rfl(lds0+PART+w*2048);
  f32x4 accA[4][4], accB[4][4]; u32x4 fS[2][4], fD[2][4];
  #pragma unroll
  for(int a=0;a<4;a++)
  #pragma unroll
  for(int b=0;b<4;b++){ accA[a][b]=(f32x4){0,0,0,0}; accB[a][b]=(f32x4){0,0,0,0}; pin(accA[a][b]); pin(accB[a][b]); }
  STAMP(1);
  if(w==0){ const int l=threadIdx.x&63; i32x4 sg=make_srd(p.ws+WS_G,S*4); set_m0<0>(rfl(lds0+GB_OFF)); nop1(); dma<0>((unsigned)(s0*4+l*16),sg,0u); }
  if(w==1){ const int l=threadIdx.x&63; i32x4 sb=make_srd(p.b2,S*2); set_m0<0>(rfl(lds0+GB_OFF+1024)); nop1(); dma<0>((unsigned)(s0*2+l*16),sb,0u); }
  auto tile=[&](auto slc, auto pac, Src& sS, Src& sD){ constexpr int SLT=decltype(slc)::value; constexpr bool PA=decltype(pac)::value!=0;
    set_m0<SLT*SLOT>(mS); nop1(); dma<0>(sS.v[0],sS.srd,sS.soff); dma<1024>(sS.v[1],sS.srd,sS.soff);
    set_m0<SLT*SLOT>(mD); nop1(); dma<0>(sD.v[0],sD.srd,sD.soff); dma<1024>(sD.v[1],sD.srd,sD.soff);
    if constexpr(PA){ saddi<128>(sS.soff); saddi<64*2048>(sD.soff); } else { saddi<64*4096>(sS.soff); saddi<128>(sD.soff); } };
  auto first_read=[&](auto pac, const Bases& B){ constexpr bool PA=decltype(pac)::value!=0;
    u32x2 l0,h0,l1,h1,l2,h2,l3,h3;
    if constexpr(PA){
      ds_b128<0>(fS[0][0],B.aR[0]); ds_b128<2048>(fS[0][1],B.aR[0]); ds_b128<4096>(fS[0][2],B.aR[0]); ds_b128<6144>(fS[0][3],B.aR[0]);
      ds_tr<PART>(l0,B.aT[0][0]); ds_tr<PART+1024>(h0,B.aT[0][0]); ds_tr<PART>(l1,B.aT[0][1]); ds_tr<PART+1024>(h1,B.aT[0][1]);
      ds_tr<PART>(l2,B.aT[0][2]); ds_tr<PART+1024>(h2,B.aT[0][2]); ds_tr<PART>(l3,B.aT[0][3]); ds_tr<PART+1024>(h3,B.aT[0][3]);
      fD[0][0]=cat(l0,h0); fD[0][1]=cat(l1,h1); fD[0][2]=cat(l2,h2); fD[0][3]=cat(l3,h3);
    } else {
      ds_b128<PART>(fD[0][0],B.aR[0]); ds_b128<PART+2048>(fD[0][1],B.aR[0]); ds_b128<PART+4096>(fD[0][2],B.aR[0]); ds_b128<PART+6144>(fD[0][3],B.aR[0]);
      ds_tr<0>(l0,B.aT[0][0]); ds_tr<1024>(h0,B.aT[0][0]); ds_tr<0>(l1,B.aT[0][1]); ds_tr<1024>(h1,B.aT[0][1]);
      ds_tr<0>(l2,B.aT[0][2]); ds_tr<1024>(h2,B.aT[0][2]); ds_tr<0>(l3,B.aT[0][3]); ds_tr<1024>(h3,B.aT[0][3]);
      fS[0][0]=cat(l0,h0); fS[0][1]=cat(l1,h1); fS[0][2]=cat(l2,h2); fS[0][3]=cat(l3,h3);
    }
    waitl<0>(); };
  #define ITA(SET,SL,RPA,DK,RB,XS,XD) iter<SET,SL,true,RPA,DK,4>(RB,mS,mD,XS,XD,accA,fS,fD)
  #define ITB(SET,SL,RD,DK,VM) iter<SET,SL,RD,false,DK,VM>(Bb,mS,mD,bS,bD,accB,fS,fD)
  Bases Bb; Src bS,bD;
  if(half==0){
    Bases Ba; Src aS,aD;
    mk_src<true>(p,aS,aD,w,s0,d0,half); mk_bases<true>(Ba,lds0,wsd,wd,kg);
    tile(IC(0),IC(1),aS,aD); tile(IC(1),IC(1),aS,aD); tile(IC(2),IC(1),aS,aD);
    waitv<8>(); bar(); first_read(IC(1),Ba);
    STAMP(2);
    for(int t=0;t<12;t+=4){ ITA(0,0,true,1,Ba,aS,aD); ITA(1,1,true,1,Ba,aS,aD); ITA(0,2,true,1,Ba,aS,aD); ITA(1,3,true,1,Ba,aS,aD); }
    ITA(0,0,true,1,Ba,aS,aD);
    mk_src<false>(p,bS,bD,w,s0,d0,half);
    ITA(1,1,true,2,Ba,bS,bD);
    ITA(0,2,true,2,Ba,bS,bD);
    mk_bases<false>(Bb,lds0,wsd,wd,kg);
    ITA(1,3,false,2,Bb,bS,bD);
  } else {
    mk_src<false>(p,bS,bD,w,s0,d0,half); mk_bases<false>(Bb,lds0,wsd,wd,kg);
    tile(IC(0),IC(0),bS,bD); tile(IC(1),IC(0),bS,bD); tile(IC(2),IC(0),bS,bD);
    waitv<8>(); bar(); first_read(IC(0),Bb);
    STAMP(2);
  }
  { const int n=half? 32 : 16;
    for(int t=0;t<n;t+=4){ ITB(0,0,true,2,4); ITB(1,1,true,2,4); ITB(0,2,true,2,4); ITB(1,3,true,2,4); } }
  ITB(0,0,true,2,4); ITB(1,1,true,0,4); ITB(0,2,true,0,0); ITB(1,3,false,0,-1);
  asm volatile("s_nop 7\n\ts_nop 7" ::: "memory");
  #pragma unroll
  for(int a=0;a<4;a++)
  #pragma unroll
  for(int b=0;b<4;b++){ pin(accA[a][b]); pin(accB[a][b]); }
  STAMP(3);
  const int tid=otid(), l=tid&63, g=l>>4, i16=l&15;
  float gt[4], bz[4];
  #pragma unroll
  for(int b=0;b<4;b++){ const int sl=64*wsd+16*b+i16; gt[b]=*(LAS float*)(sm+GB_OFF+sl*4); bz[b]=__uint_as_float(((unsigned)*(LAS u16*)(sm+GB_OFF+1024+sl*2))<<16); }
  #pragma unroll
  for(int a=0;a<4;a++)
  #pragma unroll
  for(int b=0;b<4;b++) accB[a][b]=accA[a][b]*gt[b]+accB[a][b]*(1.0f-gt[b]);
  bar();
  { LAS char* wb=sm+w*8192+l*16;
    if(kg==0){
      #pragma unroll
      for(int b=0;b<4;b++){ *(LAS f32x4*)(wb+(b*2+0)*1024)=accB[2][b]; *(LAS f32x4*)(wb+(b*2+1)*1024)=accB[3][b]; }
    } else {
      #pragma unroll
      for(int b=0;b<4;b++){ *(LAS f32x4*)(wb+(b*2+0)*1024)=accB[0][b]; *(LAS f32x4*)(wb+(b*2+1)*1024)=accB[1][b]; }
    } }
  __syncthreads();
  f32x4 r[2][4];
  { LAS char* rb=sm+(w^4)*8192+l*16;
    if(kg==0){
      #pragma unroll
      for(int b=0;b<4;b++){ r[0][b]=accB[0][b]+*(LAS f32x4*)(rb+(b*2+0)*1024); r[1][b]=accB[1][b]+*(LAS f32x4*)(rb+(b*2+1)*1024); }
    } else {
      #pragma unroll
      for(int b=0;b<4;b++){ r[0][b]=accB[2][b]+*(LAS f32x4*)(rb+(b*2+0)*1024); r[1][b]=accB[3][b]+*(LAS f32x4*)(rb+(b*2+1)*1024); }
    } }
  char* ws=p.ws;
  const int me=(st*8+dt)*2+half, other=me^1;
  const __amdgpu_buffer_rsrc_t rX=make_rsrc(ws+WS_XCH,256*32768);
  unsigned* flags=(unsigned*)(ws+WS_FLG);
  const int slot=(wd*2+kg)*8192+l*16;
  if(wsd!=half){
    #pragma unroll
    for(int b=0;b<4;b++){ st_sc1(rX,(unsigned)(me*32768+slot+(b*2+0)*1024),__builtin_bit_cast(u32x4,r[0][b])); st_sc1(rX,(unsigned)(me*32768+slot+(b*2+1)*1024),__builtin_bit_cast(u32x4,r[1][b])); }
  }
  asm volatile("s_waitcnt vmcnt(0)" ::: "memory");
  __syncthreads();
  if(tid==0) flag_st(flags+(FLG_X+me)*16, epoch);
  if(w==0) poll_flags(flags, FLG_X+other, 1, epoch, l);
  __syncthreads();
  STAMP(4);
  if(wsd==half){
    u32x4 t[2][4];
    #pragma unroll
    for(int b=0;b<4;b++){ t[0][b]=ld_sc1(rX,(unsigned)(other*32768+slot+(b*2+0)*1024)); t[1][b]=ld_sc1(rX,(unsigned)(other*32768+slot+(b*2+1)*1024)); }
    const int offg=(g&1)*16+(g>>1)*8;
    #pragma unroll
    for(int b=0;b<4;b++){
      const float add=(1.0f-gt[b])*bz[b];
      const f32x4 x0=r[0][b]+__builtin_bit_cast(f32x4,t[0][b])+add, x1=r[1][b]+__builtin_bit_cast(f32x4,t[1][b])+add;
      unsigned a0=cvtpk(x0[0],x0[1]), a1=cvtpk(x0[2],x0[3]), b0=cvtpk(x1[0],x1[1]), b1=cvtpk(x1[2],x1[3]);
      auto r0=__builtin_amdgcn_permlane16_swap(a0,b0,false,false); auto r1=__builtin_amdgcn_permlane16_swap(a1,b1,false,false);
      const u32x4 ov={r0[0],r1[0],r0[1],r1[1]};
      const int s=s0+64*wsd+16*b+i16;
      *(u32x4*)(p.out+(size_t)s*D+d0+64*wd+32*kg+offg)=ov;
    }
  }
  STAMP(5);
}
}

__global__ __launch_bounds__(512,2) void k_final(Params p){
  extern __shared__ __attribute__((aligned(16))) char smem[];
  unsigned long long* stamps=(unsigned long long*)(p.ws+WS_STM)+4096;
  (void)stamps;
  RSTAMP(0);
  const int b=blockIdx.x, xcd=b&7, j=b>>3;
  const int half=xcd&1, grp=xcd>>1;
  const int st=(grp&1)*8+(j&7), dt=(grp>>1)*4+(j>>3);
  fin::run(p,smem,st,dt,half,1u,stamps);
  RSTAMP(15);
}

extern "C" void kernel_launch(void* const* d_in, const int* in_sizes, int n_in, void* d_out, int out_size, void* d_ws, size_t ws_size, hipStream_t stream){
  (void)in_sizes; (void)n_in; (void)out_size; (void)ws_size;
  Params p;
  p.x=(const u16*)d_in[0]; p.wqkv=(const u16*)d_in[1]; p.wphi=(const u16*)d_in[2]; p.wo=(const u16*)d_in[3];
  p.w1=(const u16*)d_in[5]; p.b1=(const u16*)d_in[6]; p.w2=(const u16*)d_in[7]; p.b2=(const u16*)d_in[8]; p.gw=(const u16*)d_in[9];
  p.out=(u16*)d_out; p.ws=(char*)d_ws;
  constexpr int LDS_A = (qkv::LDS_BYTES>mix1::LDS_BYTES)?qkv::LDS_BYTES:mix1::LDS_BYTES;
  (void)hipFuncSetAttribute((const void*)k_phaseA, hipFuncAttributeMaxDynamicSharedMemorySize, LDS_A);
  (void)hipFuncSetAttribute((const void*)k_final, hipFuncAttributeMaxDynamicSharedMemorySize, fin::LDS_BYTES);
  k_ln<<<256,512,0,stream>>>(p);
  k_phaseA<<<256,512,LDS_A,stream>>>(p);
  k_final<<<256,512,fin::LDS_BYTES,stream>>>(p);
}
```

```cpp
#include <hip/hip_runtime.h>
#include <hip/hip_bf16.h>
#include <type_traits>
#include <utility>

#define LAS __attribute__((address_space(3)))
#define DI __device__ __forceinline__
#define IC(n) std::integral_constant<int,(n)>{}
typedef unsigned short u16;
typedef float f32x4 __attribute__((ext_vector_type(4)));
typedef float f32x2 __attribute__((ext_vector_type(2)));
typedef int i32x4 __attribute__((ext_vector_type(4)));
typedef unsigned u32x4 __attribute__((ext_vector_type(4)));
typedef unsigned u32x2 __attribute__((ext_vector_type(2)));
typedef short bf16x8 __attribute__((ext_vector_type(8)));
typedef __bf16 bf16x2_t __attribute__((ext_vector_type(2)));

constexpr int S=2048, D=1024, F=3584;
#define PF_LATE 1
#define PFQ0 4
#define PFM0 5
constexpr size_t WS_H   = 0;
constexpr size_t WS_U   = WS_H + (size_t)S*D*2;
constexpr size_t WS_O   = WS_U + (size_t)D*F*2;
constexpr size_t WS_KVP = WS_O + (size_t)S*D*2;
constexpr size_t WS_ZP  = WS_KVP + (size_t)16*8*8*2048;
constexpr size_t WS_G   = WS_ZP + (size_t)16*8*2*64*4;
constexpr size_t WS_XCH = WS_G + 8192;
constexpr size_t WS_FLG = WS_XCH + (size_t)256*32768;
constexpr size_t WS_STM = WS_FLG + 65536;
constexpr size_t WS_END = WS_STM + 65536+262144;
constexpr int FLG_KV = 0;
constexpr int FLG_X  = 128;

template<int OFF> DI void ds_tr(u32x2& d, unsigned a){ asm volatile("ds_read_b64_tr_b16 %0, %1 offset:%2" : "=v"(d) : "v"(a), "i"(OFF)); }
template<int OFF> DI void ds_b128(u32x4& d, unsigned a){ asm volatile("ds_read_b128 %0, %1 offset:%2" : "=v"(d) : "v"(a), "i"(OFF)); }
DI void mfma(f32x4& c, const u32x4& a, const u32x4& b){ asm volatile("v_mfma_f32_16x16x32_bf16 %0, %1, %2, %0" : "+v"(c) : "v"(a), "v"(b)); }
DI f32x4 mfma_b(const u32x4& a, const u32x4& b, const f32x4& c){ return __builtin_amdgcn_mfma_f32_16x16x32_bf16(__builtin_bit_cast(bf16x8,a), __builtin_bit_cast(bf16x8,b), c, 0,0,0); }
template<int IMM> DI void dma(unsigned voff, const i32x4& srd, unsigned soff){
  asm volatile("buffer_load_dwordx4 %0, %1, %2 offen offset:%3 lds" :: "v"(voff), "s"(srd), "s"(soff), "i"(IMM) : "memory"); }
template<int V> DI void set_m0(unsigned base){ asm volatile("s_add_u32 m0, %0, %1" :: "s"(base), "i"(V) : "scc"); }
struct Pf { i32x4 srd; unsigned voff, soff, step, m0; };
DI void pf_m0(const Pf& f){ asm volatile("s_mov_b32 m0, %0" :: "s"(f.m0)); }
DI void pf_touch_(Pf& f){ asm volatile("buffer_load_dword %1, %2, %0 offen lds\n\ts_add_u32 %0, %0, %3" : "+s"(f.soff) : "v"(f.voff), "s"(f.srd), "s"(f.step) : "memory", "scc"); }
#define PF1_ON 1
#define PF2_ON 1
DI void pf_touch(Pf& f){ if constexpr(PF1_ON) pf_touch_(f); }
DI void pf_touch2(Pf& f){ if constexpr(PF2_ON) pf_touch_(f); }
DI void nop1(){ asm volatile("s_nop 0"); }
template<int N> DI void waitv(){ asm volatile("s_waitcnt vmcnt(%0)" :: "n"(N) : "memory"); }
template<int N> DI void waitl(){ asm volatile("s_waitcnt lgkmcnt(%0)" :: "n"(N) : "memory"); }
DI void bar(){ asm volatile("s_barrier" ::: "memory"); }
template<int V> DI void vaddi(unsigned& x){ asm volatile("v_add_u32 %0, %1, %0" : "+v"(x) : "i"(V)); }
template<int V> DI void saddi(unsigned& x){ asm volatile("s_add_u32 %0, %0, %1" : "+s"(x) : "i"(V) : "scc"); }
DI void pin(f32x4& x){ asm volatile("" : "+v"(x)); }
DI u32x4 cat(u32x2 lo,u32x2 hi){ return __builtin_shufflevector(lo,hi,0,1,2,3); }
DI unsigned cvtpk(float lo,float hi){ f32x2 v={lo,hi}; bf16x2_t b=__builtin_convertvector(v,bf16x2_t); return __builtin_bit_cast(unsigned,b); }
DI float bflo(unsigned x){ return __uint_as_float(x<<16); }
DI float bfhi(unsigned x){ return __uint_as_float(x&0xffff0000u); }
DI int otid(){ int t=threadIdx.x; asm volatile("" : "+v"(t)); return t; }
DI unsigned rfl(unsigned x){ return __builtin_amdgcn_readfirstlane(x); }
DI i32x4 make_srd(const void* p, unsigned bytes){
  unsigned long long a=(unsigned long long)p; i32x4 s;
  s[0]=(int)rfl((unsigned)a); s[1]=(int)(rfl((unsigned)(a>>32))&0xffff); s[2]=(int)bytes; s[3]=0x00020000; return s; }
DI __amdgpu_buffer_rsrc_t make_rsrc(const void* p, unsigned bytes){ return __builtin_amdgcn_make_buffer_rsrc((void*)p, 0, bytes, 0x00020000); }
DI void st_sc1(__amdgpu_buffer_rsrc_t r, unsigned off, const u32x4& v){ __builtin_amdgcn_raw_buffer_store_b128(v, r, off, 0, 16); }
DI u32x4 ld_sc1(__amdgpu_buffer_rsrc_t r, unsigned off){ return __builtin_amdgcn_raw_buffer_load_b128(r, off, 0, 16); }
DI float xr16_max(float x){ auto r=__builtin_amdgcn_permlane16_swap(__float_as_uint(x),__float_as_uint(x),false,false); return fmaxf(__uint_as_float(r[0]),__uint_as_float(r[1])); }
DI float xr32_max(float x){ auto r=__builtin_amdgcn_permlane32_swap(__float_as_uint(x),__float_as_uint(x),false,false); return fmaxf(__uint_as_float(r[0]),__uint_as_float(r[1])); }
DI float xr16_min(float x){ auto r=__builtin_amdgcn_permlane16_swap(__float_as_uint(x),__float_as_uint(x),false,false); return fminf(__uint_as_float(r[0]),__uint_as_float(r[1])); }
DI float xr32_min(float x){ auto r=__builtin_amdgcn_permlane32_swap(__float_as_uint(x),__float_as_uint(x),false,false); return fminf(__uint_as_float(r[0]),__uint_as_float(r[1])); }
DI float xr16_add(float x){ auto r=__builtin_amdgcn_permlane16_swap(__float_as_uint(x),__float_as_uint(x),false,false); return __uint_as_float(r[0])+__uint_as_float(r[1]); }
DI float xr32_add(float x){ auto r=__builtin_amdgcn_permlane32_swap(__float_as_uint(x),__float_as_uint(x),false,false); return __uint_as_float(r[0])+__uint_as_float(r[1]); }
DI unsigned flag_ld(const unsigned* p){ return __hip_atomic_load(p, __ATOMIC_RELAXED, __HIP_MEMORY_SCOPE_AGENT); }
DI void flag_st(unsigned* p, unsigned v){ __hip_atomic_store(p, v, __ATOMIC_RELAXED, __HIP_MEMORY_SCOPE_AGENT); }
DI void poll_flags(const unsigned* flags, int first, int n, unsigned epoch, int lane){
  const unsigned* p = flags + (size_t)(first + (lane<n?lane:0))*16;
  for(unsigned spins=0; spins<(1u<<22); ++spins){
    unsigned v=flag_ld(p);
    if(__all(v==epoch)) break;
    __builtin_amdgcn_s_sleep(1);
  }
}
#define STAMP(i)
#define RSTAMP(i)

struct Params {
  const u16* x; const u16* wqkv; const u16* wphi; const u16* wo; const u16* w1; const u16* b1; const u16* w2; const u16* b2; const u16* gw;
  u16* out; char* ws;
};

__global__ __launch_bounds__(512) void k_ln(Params p){
  const int tid=threadIdx.x, w=tid>>6, l=tid&63;
  const int s=blockIdx.x*8+w;
  if(blockIdx.x==0){ unsigned* fl=(unsigned*)(p.ws+WS_FLG); for(int i=tid;i<512*16;i+=512) fl[i]=0; }
  const u32x4 a0=*(const u32x4*)(p.x+(size_t)s*D+l*8), a1=*(const u32x4*)(p.x+(size_t)s*D+512+l*8);
  const u32x4 g0=*(const u32x4*)(p.gw+l*8), g1=*(const u32x4*)(p.gw+512+l*8);
  unsigned pfv=0;
  if(tid<42) pfv=*(const unsigned*)((const char*)p.w1+(size_t)(blockIdx.x*42+tid)*128);
  else if(tid>=64 && tid<88) pfv=*(const unsigned*)((const char*)p.wqkv+(size_t)(blockIdx.x*24+tid-64)*128);
  float v[16], gv[16];
  #pragma unroll
  for(int i=0;i<4;i++){ v[2*i]=bflo(a0[i]); v[2*i+1]=bfhi(a0[i]); v[8+2*i]=bflo(a1[i]); v[8+2*i+1]=bfhi(a1[i]);
                        gv[2*i]=bflo(g0[i]); gv[2*i+1]=bfhi(g0[i]); gv[8+2*i]=bflo(g1[i]); gv[8+2*i+1]=bfhi(g1[i]); }
  float sum=0.f, z=0.f;
  #pragma unroll
  for(int i=0;i<16;i++){ sum+=v[i]; z+=v[i]*gv[i]; }
  #pragma unroll
  for(int o=1;o<64;o<<=1){ sum+=__shfl_xor(sum,o); z+=__shfl_xor(z,o); }
  const float mean=sum*(1.0f/D);
  float q=0.f;
  #pragma unroll
  for(int i=0;i<16;i++){ v[i]-=mean; q+=v[i]*v[i]; }
  #pragma unroll
  for(int o=1;o<64;o<<=1) q+=__shfl_xor(q,o);
  const float r=1.0f/sqrtf(q*(1.0f/D)+1e-5f);
  u32x4 o0,o1;
  #pragma unroll
  for(int i=0;i<4;i++){ o0[i]=cvtpk(v[2*i]*r,v[2*i+1]*r); o1[i]=cvtpk(v[8+2*i]*r,v[8+2*i+1]*r); }
  u16* h=(u16*)(p.ws+WS_H);
  *(u32x4*)(h+(size_t)s*D+l*8)=o0; *(u32x4*)(h+(size_t)s*D+512+l*8)=o1;
  if(l==0) ((float*)(p.ws+WS_G))[s]=1.0f/(1.0f+__expf(-z));
  asm volatile("" :: "v"(pfv));
}

namespace qkv {
constexpr int XPART=256*128, WPART=64*384, SLOT=XPART+WPART;
constexpr int WPHI_OFF=2*SLOT;
constexpr int PFD_OFF=WPHI_OFF+4096;
constexpr int LDS_BYTES=PFD_OFF+256;
struct Ctx { unsigned aX[2][2], aW[2][2], vD[8]; i32x4 srdD; unsigned mD, soffD, stepD; Pf pf, pf2; };

template<bool ISPF,int SET,int RSLOT,int RKS,bool READ,bool DMA,int DSLOT>
DI void step(Ctx& c, f32x4 (&acc)[3][2][4], u32x4 (&fX)[2][4], u32x4 (&fW)[2][3][2]){
  constexpr int NX=SET^1;
  u32x2 wl[3][2], wh[3][2];
  auto RX=[&](auto mc){ constexpr int m=decltype(mc)::value; if constexpr(READ) ds_b128<m*2048>(fX[NX][m], c.aX[RSLOT][RKS]); };
  auto RW=[&](auto sc,auto ec,auto tc){ constexpr int s=decltype(sc)::value,e=decltype(ec)::value,t=decltype(tc)::value;
    if constexpr(READ){ if constexpr(t==0) ds_tr<RKS*12288+s*1024>(wl[s][e], c.aW[RSLOT][e]); else { ds_tr<RKS*12288+s*1024+512>(wh[s][e], c.aW[RSLOT][e]); fW[NX][s][e]=cat(wl[s][e],wh[s][e]); } } };
  auto M0=[&](auto hc){ constexpr int h=decltype(hc)::value; if constexpr(DMA && !ISPF) set_m0<DSLOT*SLOT+h*4096>(c.mD); if constexpr(DMA && ISPF && h==0) pf_m0(c.pf); };
  auto DD=[&](auto kc){ constexpr int k=decltype(kc)::value; if constexpr(DMA && !ISPF) dma<(k&3)*1024>(c.vD[k], c.srdD, c.soffD);

                        };
  auto MM=[&](auto sc,auto ec,auto mc){ constexpr int s=decltype(sc)::value,e=decltype(ec)::value,m=decltype(mc)::value;
    if constexpr(s<2) mfma(acc[s][e][m], fW[SET][s][e], fX[SET][m]); else mfma(acc[s][e][m], fX[SET][m], fW[SET][s][e]); };
  MM(IC(0),IC(0),IC(0)); RX(IC(0)); M0(IC(0));
  MM(IC(0),IC(0),IC(1)); RW(IC(0),IC(0),IC(0)); DD(IC(0));
  MM(IC(0),IC(0),IC(2)); RW(IC(0),IC(0),IC(1));
  MM(IC(0),IC(0),IC(3)); RX(IC(1)); DD(IC(1));
  MM(IC(0),IC(1),IC(0)); RW(IC(0),IC(1),IC(0));
  MM(IC(0),IC(1),IC(1)); RW(IC(0),IC(1),IC(1)); DD(IC(2));
  MM(IC(0),IC(1),IC(2)); RX(IC(2));
  MM(IC(0),IC(1),IC(3)); RX(IC(3)); DD(IC(3));
  MM(IC(1),IC(0),IC(0)); RW(IC(1),IC(0),IC(0)); M0(IC(1));
  MM(IC(1),IC(0),IC(1)); RW(IC(1),IC(0),IC(1)); DD(IC(4));
  MM(IC(1),IC(0),IC(2)); RW(IC(1),IC(1),IC(0));
  MM(IC(1),IC(0),IC(3)); RW(IC(1),IC(1),IC(1)); DD(IC(5));
  MM(IC(1),IC(1),IC(0)); RW(IC(2),IC(0),IC(0));
  MM(IC(1),IC(1),IC(1)); RW(IC(2),IC(0),IC(1)); DD(IC(6));
  MM(IC(1),IC(1),IC(2)); RW(IC(2),IC(1),IC(0));
  MM(IC(1),IC(1),IC(3)); RW(IC(2),IC(1),IC(1)); DD(IC(7));
  MM(IC(2),IC(0),IC(0));
  if constexpr(DMA && !ISPF) asm volatile("s_add_u32 %0, %0, %1" : "+s"(c.soffD) : "s"(c.stepD) : "scc");
  MM(IC(2),IC(0),IC(1)); MM(IC(2),IC(0),IC(2)); MM(IC(2),IC(0),IC(3));
  MM(IC(2),IC(1),IC(0)); MM(IC(2),IC(1),IC(1)); MM(IC(2),IC(1),IC(2));
  if constexpr(DMA && ISPF){ pf_touch(c.pf); pf_touch2(c.pf2); }
  MM(IC(2),IC(1),IC(3));
  if constexpr(READ) waitl<0>();
}
template<bool ISPF,int SL,bool NEXT,bool DMA>
DI void iter(Ctx& c, f32x4 (&acc)[3][2][4], u32x4 (&fX)[2][4], u32x4 (&fW)[2][3][2]){
  step<ISPF,0,SL,1,true,false,0>(c,acc,fX,fW);
  if constexpr(NEXT){ if constexpr(!ISPF) waitv<0>(); bar(); }
  step<ISPF,1,SL^1,0,NEXT,DMA,SL>(c,acc,fX,fW);
}

DI void epilogue(const Params& p, char* smem, f32x4 (&acc)[3][2][4], int rt, int ct, int w, unsigned epoch, unsigned long long* stamps);
template<bool ISPF>
DI void run(const Params& p, char* smem, int rt, int ct, unsigned epoch, unsigned long long* stamps){
  const unsigned lds0=(unsigned)(size_t)(LAS char*)smem;
  const int tid=threadIdx.x, w=ISPF?7:rfl(tid>>6), l=tid&63;
  const int g=l>>4,q=(l&15)>>2,pp=l&3,i16=l&15;
  const int wr=w&3, wh=w>>2;
  Ctx c; f32x4 acc[3][2][4]; u32x4 fX[2][4], fW[2][3][2];
  if constexpr(!ISPF){
    c.mD=rfl(lds0+w*8192);
    if(w<4){
      c.srdD=make_srd(p.x,S*D*2); c.stepD=128;
      #pragma unroll
      for(int k=0;k<8;k++){ const int row=64*w+8*k+(l>>3); const int ch=(l&7)^((row>>1)&7); c.vD[k]=(rt*256+row)*2048+ch*16-1024*(k&3); }
    } else {
      c.srdD=make_srd(p.wqkv,D*3*D*2); c.stepD=64*6144;
      #pragma unroll
      for(int k=0;k<8;k++){ const int j=(w-4)*8+k, rb=j/3, s=j-rb*3; const int row=8*rb+(l>>3); const int sw=((row>>1)&1)|(((row>>3)&1)<<1); const int pos=l&7; const int ch=(((pos>>1)^sw)<<1)|(pos&1);
        c.vD[k]=row*6144+s*2048+ct*128+ch*16-1024*(k&3); }
    }
    c.stepD=rfl(c.stepD); c.soffD=0;
    #pragma unroll
    for(int i=0;i<4;i++) c.srdD[i]=(int)rfl((unsigned)c.srdD[i]);
  } else {
    c.pf.srd=make_srd(p.wqkv,D*3*D*2); { const int it=rt*24+l; c.pf.voff=(l<24)? (unsigned)((it/3)*6144+(it%3)*2048+ct*128) : 0x80000000u; } c.pf.soff=2*64*6144; c.pf.step=64*6144; c.pf.m0=rfl(lds0+PFD_OFF);
    c.pf2.srd=make_srd(p.wo,D*D*2); c.pf2.voff=(l<8)? (unsigned)(((ct*8+rt)*8+l)*128) : 0x80000000u; c.pf2.soff=0; c.pf2.step=128*8*128; c.pf2.m0=c.pf.m0;
  }
  #pragma unroll
  for(int sl=0;sl<2;sl++){
    #pragma unroll
    for(int ks=0;ks<2;ks++) c.aX[sl][ks]=lds0+sl*SLOT+(64*wr+i16)*128+(((4*ks+g)^((i16>>1)&7))*16);
    const int sw2=((q>>1)&1)|((g&1)<<1);
    #pragma unroll
    for(int e=0;e<2;e++) c.aW[sl][e]=lds0+sl*SLOT+XPART+g*3072+q*128+(((2*wh+e)^sw2)*32)+8*pp;
  }
  #pragma unroll
  for(int s=0;s<3;s++)
  #pragma unroll
  for(int e=0;e<2;e++)
  #pragma unroll
  for(int m=0;m<4;m++){ acc[s][e][m]=(f32x4){0,0,0,0}; pin(acc[s][e][m]); }
  STAMP(1);
  if constexpr(!ISPF){
    if(w<4){ i32x4 srdP=make_srd(p.wphi,32*32*32*2); set_m0<0>(rfl(lds0+WPHI_OFF+w*1024)); nop1(); dma<0>((unsigned)(ct*4096+w*1024+l*16), srdP, 0u); }
    auto tile=[&](auto tc){ constexpr int T=decltype(tc)::value;
      set_m0<T*SLOT>(c.mD); nop1(); dma<0>(c.vD[0],c.srdD,c.soffD); dma<1024>(c.vD[1],c.srdD,c.soffD); dma<2048>(c.vD[2],c.srdD,c.soffD); dma<3072>(c.vD[3],c.srdD,c.soffD);
      set_m0<T*SLOT+4096>(c.mD); nop1(); dma<0>(c.vD[4],c.srdD,c.soffD); dma<1024>(c.vD[5],c.srdD,c.soffD); dma<2048>(c.vD[6],c.srdD,c.soffD); dma<3072>(c.vD[7],c.srdD,c.soffD);
      asm volatile("s_add_u32 %0, %0, %1" : "+s"(c.soffD) : "s"(c.stepD) : "scc"); };
    tile(IC(0)); tile(IC(1));
    waitv<8>();
  } else {
    pf_m0(c.pf); nop1();
    #pragma unroll
    for(int k=0;k<PFQ0;k++) pf_touch(c.pf);
  }
  bar();
  {
    ds_b128<0>(fX[0][0],c.aX[0][0]); ds_b128<2048>(fX[0][1],c.aX[0][0]); ds_b128<4096>(fX[0][2],c.aX[0][0]); ds_b128<6144>(fX[0][3],c.aX[0][0]);
    auto rd=[&](auto sc,auto ec){ constexpr int s=decltype(sc)::value,e=decltype(ec)::value; u32x2 lo,hi; ds_tr<s*1024>(lo,c.aW[0][e]); ds_tr<s*1024+512>(hi,c.aW[0][e]); fW[0][s][e]=cat(lo,hi); };
    rd(IC(0),IC(0)); rd(IC(0),IC(1)); rd(IC(1),IC(0)); rd(IC(1),IC(1)); rd(IC(2),IC(0)); rd(IC(2),IC(1));
    waitl<0>(); }
  STAMP(2);
  for(int t=0;t<14;t+=2){ iter<ISPF,0,true,true>(c,acc,fX,fW); iter<ISPF,1,true,true>(c,acc,fX,fW); }
  iter<ISPF,0,true,false>(c,acc,fX,fW); iter<ISPF,1,false,false>(c,acc,fX,fW);
  asm volatile("s_nop 7\n\ts_nop 7" ::: "memory");
  #pragma unroll
  for(int s=0;s<3;s++)
  #pragma unroll
  for(int e=0;e<2;e++)
  #pragma unroll
  for(int m=0;m<4;m++) pin(acc[s][e][m]);
  STAMP(3);
  epilogue(p,smem,acc,rt,ct,w,epoch,stamps);
}
DI void epilogue(const Params& p, char* smem, f32x4 (&acc)[3][2][4], int rt, int ct, int w, unsigned epoch, unsigned long long* stamps){
  const unsigned lds0=(unsigned)(size_t)(LAS char*)smem;
  LAS char* sm=(LAS char*)smem;
  const int tid=otid(), l=tid&63;
  const int g=l>>4,q=(l&15)>>2,pp=l&3,i16=l&15;
  const int wr=w&3, wh=w>>2;
  constexpr int TPHI=0, VREG=65536, SHR=98304;
  u32x4 Ap[2];
  { const unsigned b=lds0+WPHI_OFF+wh*2048+(4*g+q)*64+pp*8;
    u32x2 l0,h0,l1,h1; ds_tr<0>(l0,b); ds_tr<1024>(h0,b); ds_tr<32>(l1,b); ds_tr<32+1024>(h1,b); waitl<0>();
    asm volatile("" : "+v"(l0),"+v"(h0),"+v"(l1),"+v"(h1));
    Ap[0]=cat(l0,h0); Ap[1]=cat(l1,h1); }
  constexpr float L2E=1.4426950408889634f;
  u32x4 phq[4][2];
  const int swp=(((i16>>1)&3)<<2)|(((i16>>3)&1)<<1)|(i16&1);
  #pragma unroll
  for(int X=0;X<2;X++){
    #pragma unroll
    for(int m=0;m<4;m++){
      u32x4 B; B[0]=cvtpk(acc[X][0][m][0],acc[X][0][m][1]); B[1]=cvtpk(acc[X][0][m][2],acc[X][0][m][3]); B[2]=cvtpk(acc[X][1][m][0],acc[X][1][m][1]); B[3]=cvtpk(acc[X][1][m][2],acc[X][1][m][3]);
      const f32x4 zero={0,0,0,0};
      f32x4 P0=mfma_b(Ap[0],B,zero), P1=mfma_b(Ap[1],B,zero);
      float mx=fmaxf(fmaxf(fmaxf(P0[0],P0[1]),fmaxf(P0[2],P0[3])),fmaxf(fmaxf(P1[0],P1[1]),fmaxf(P1[2],P1[3])));
      float mn=fminf(fminf(fminf(P0[0],P0[1]),fminf(P0[2],P0[3])),fminf(fminf(P1[0],P1[1]),fminf(P1[2],P1[3])));
      mx=xr32_max(xr16_max(mx)); mn=xr32_min(xr16_min(mn));
      const float mxl=mx*L2E, mnl=mn*L2E;
      float ep[8], en[8];
      #pragma unroll
      for(int i=0;i<4;i++){ ep[i]=__builtin_amdgcn_exp2f(P0[i]*L2E-mxl); ep[4+i]=__builtin_amdgcn_exp2f(P1[i]*L2E-mxl); en[i]=__builtin_amdgcn_exp2f(mnl-P0[i]*L2E); en[4+i]=__builtin_amdgcn_exp2f(mnl-P1[i]*L2E); }
      float sp=((ep[0]+ep[1])+(ep[2]+ep[3]))+((ep[4]+ep[5])+(ep[6]+ep[7])), sn=((en[0]+en[1])+(en[2]+en[3]))+((en[4]+en[5])+(en[6]+en[7]));
      sp=xr32_add(xr16_add(sp)); sn=xr32_add(xr16_add(sn));
      const float rp=__builtin_amdgcn_rcpf(sp), rn=__builtin_amdgcn_rcpf(sn);
      u32x4 hp,hn;
      hp[0]=cvtpk(ep[0]*rp,ep[1]*rp); hp[1]=cvtpk(ep[2]*rp,ep[3]*rp); hp[2]=cvtpk(ep[4]*rp,ep[5]*rp); hp[3]=cvtpk(ep[6]*rp,ep[7]*rp);
      hn[0]=cvtpk(en[0]*rn,en[1]*rn); hn[1]=cvtpk(en[2]*rn,en[3]*rn); hn[2]=cvtpk(en[4]*rn,en[5]*rn); hn[3]=cvtpk(en[6]*rn,en[7]*rn);
      if(X==0){ phq[m][0]=hp; phq[m][1]=hn; }
      else {
        LAS char* row=sm+TPHI+w*8192+(16*m+i16)*128;
        *(LAS u32x2*)(row+(((0+g)^swp)*8))=(u32x2){hp[0],hp[1]};
        *(LAS u32x2*)(row+(((4+g)^swp)*8))=(u32x2){hp[2],hp[3]};
        *(LAS u32x2*)(row+(((8+g)^swp)*8))=(u32x2){hn[0],hn[1]};
        *(LAS u32x2*)(row+(((12+g)^swp)*8))=(u32x2){hn[2],hn[3]};
      }
    }
  }
  #pragma unroll
  for(int mp=0;mp<2;mp++)
  #pragma unroll
  for(int e=0;e<2;e++){
    u32x4 fv; fv[0]=cvtpk(acc[2][e][2*mp][0],acc[2][e][2*mp][1]); fv[1]=cvtpk(acc[2][e][2*mp][2],acc[2][e][2*mp][3]); fv[2]=cvtpk(acc[2][e][2*mp+1][0],acc[2][e][2*mp+1][1]); fv[3]=cvtpk(acc[2][e][2*mp+1][2],acc[2][e][2*mp+1][3]);
    *(LAS u32x4*)(sm+VREG+w*4096+(mp*2+e)*1024+l*16)=fv;
  }
  __syncthreads();
  STAMP(4);
  f32x4 kv0={0,0,0,0},kv1={0,0,0,0},zz={0,0,0,0};
  { const u32x4 ones={0x3F803F80u,0x3F803F80u,0x3F803F80u,0x3F803F80u};
    const int r15=4*g+q; const int swr=(((r15>>1)&3)<<2)|(((r15>>3)&1)<<1)|(r15&1);
    const unsigned ab=lds0+TPHI+wh*4*8192+r15*128+(((4*wr+pp)^swr)*8);
    #pragma unroll
    for(int sw_=0;sw_<4;sw_++){
      u32x2 a0,a1,a2,a3;
      if(sw_==0){ ds_tr<0*8192+0>(a0,ab); ds_tr<0*8192+2048>(a1,ab); ds_tr<0*8192+4096>(a2,ab); ds_tr<0*8192+6144>(a3,ab); }
      if(sw_==1){ ds_tr<1*8192+0>(a0,ab); ds_tr<1*8192+2048>(a1,ab); ds_tr<1*8192+4096>(a2,ab); ds_tr<1*8192+6144>(a3,ab); }
      if(sw_==2){ ds_tr<2*8192+0>(a0,ab); ds_tr<2*8192+2048>(a1,ab); ds_tr<2*8192+4096>(a2,ab); ds_tr<2*8192+6144>(a3,ab); }
      if(sw_==3){ ds_tr<3*8192+0>(a0,ab); ds_tr<3*8192+2048>(a1,ab); ds_tr<3*8192+4096>(a2,ab); ds_tr<3*8192+6144>(a3,ab); }
      LAS char* vb=sm+VREG+(wh*4+sw_)*4096+l*16;
      u32x4 v00=*(LAS u32x4*)(vb), v01=*(LAS u32x4*)(vb+1024), v10=*(LAS u32x4*)(vb+2048), v11=*(LAS u32x4*)(vb+3072);
      asm volatile("s_waitcnt lgkmcnt(0)" : "+v"(a0),"+v"(a1),"+v"(a2),"+v"(a3) :: "memory");
      const u32x4 A0=cat(a0,a1), A1=cat(a2,a3);
      kv0=mfma_b(A0,v00,kv0); kv1=mfma_b(A0,v01,kv1); zz=mfma_b(A0,ones,zz);
      kv0=mfma_b(A1,v10,kv0); kv1=mfma_b(A1,v11,kv1); zz=mfma_b(A1,ones,zz);
    }
  }
  char* ws=p.ws;
  const __amdgpu_buffer_rsrc_t rKV=make_rsrc(ws+WS_KVP,16*8*8*2048), rZ=make_rsrc(ws+WS_ZP,16*8*2*64*4);
  st_sc1(rKV,(unsigned)(((ct*8+rt)*8+w)*2048+l*16),__builtin_bit_cast(u32x4,kv0));
  st_sc1(rKV,(unsigned)(((ct*8+rt)*8+w)*2048+1024+l*16),__builtin_bit_cast(u32x4,kv1));
  if(i16==0) st_sc1(rZ,(unsigned)((((ct*8+rt)*2+wh)*64+16*wr+4*g)*4),__builtin_bit_cast(u32x4,zz));
  asm volatile("s_waitcnt vmcnt(0)" ::: "memory");
  __syncthreads();
  unsigned* flags=(unsigned*)(ws+WS_FLG);
  if(tid==0) flag_st(flags+(FLG_KV+ct*8+rt)*16, epoch);
  if(w==0) poll_flags(flags, FLG_KV+ct*8, 8, epoch, l);
  __syncthreads();
  STAMP(5);
  { f32x4 s0={0,0,0,0},s1={0,0,0,0},sz={0,0,0,0};
    u32x4 t0[8],t1[8],tz[8];
    #pragma unroll
    for(int r=0;r<8;r++){ t0[r]=ld_sc1(rKV,(unsigned)(((ct*8+r)*8+w)*2048+l*16)); t1[r]=ld_sc1(rKV,(unsigned)(((ct*8+r)*8+w)*2048+1024+l*16)); tz[r]=ld_sc1(rZ,(unsigned)((((ct*8+r)*2+wh)*64+16*wr+4*g)*4)); }
    #pragma unroll
    for(int r=0;r<8;r++){ s0+=__builtin_bit_cast(f32x4,t0[r]); s1+=__builtin_bit_cast(f32x4,t1[r]); sz+=__builtin_bit_cast(f32x4,tz[r]); }
    LAS char* sb=sm+SHR+w*1536+l*8;
    *(LAS u32x2*)(sb)=(u32x2){cvtpk(s0[0],s0[1]),cvtpk(s0[2],s0[3])};
    *(LAS u32x2*)(sb+512)=(u32x2){cvtpk(s1[0],s1[1]),cvtpk(s1[2],s1[3])};
    *(LAS u32x2*)(sb+1024)=(u32x2){cvtpk(sz[0],sz[1]),cvtpk(sz[2],sz[3])};
  }
  __syncthreads();
  { u32x4 kf[2][2], zf[2];
    #pragma unroll
    for(int sg=0;sg<2;sg++){
      LAS char* b0=sm+SHR+(wh*4+2*sg)*1536+l*8; LAS char* b1=b0+1536;
      kf[sg][0]=cat(*(LAS u32x2*)(b0),*(LAS u32x2*)(b1)); kf[sg][1]=cat(*(LAS u32x2*)(b0+512),*(LAS u32x2*)(b1+512)); zf[sg]=cat(*(LAS u32x2*)(b0+1024),*(LAS u32x2*)(b1+1024)); }
    const __amdgpu_buffer_rsrc_t rO=make_rsrc(ws+WS_O,S*D*2);
    const int offg=(g&1)*16+(g>>1)*8;
    #pragma unroll
    for(int m=0;m<4;m++){
      const f32x4 zero={0,0,0,0};
      f32x4 n0=mfma_b(kf[0][0],phq[m][0],zero); n0=mfma_b(kf[1][0],phq[m][1],n0);
      f32x4 n1=mfma_b(kf[0][1],phq[m][0],zero); n1=mfma_b(kf[1][1],phq[m][1],n1);
      f32x4 dn=mfma_b(zf[0],phq[m][0],zero); dn=mfma_b(zf[1],phq[m][1],dn);
      const float rd=1.0f/(dn[0]+1e-6f);
      unsigned a0=cvtpk(n0[0]*rd,n0[1]*rd), a1=cvtpk(n0[2]*rd,n0[3]*rd), b0=cvtpk(n1[0]*rd,n1[1]*rd), b1=cvtpk(n1[2]*rd,n1[3]*rd);
      auto r0=__builtin_amdgcn_permlane16_swap(a0,b0,false,false); auto r1=__builtin_amdgcn_permlane16_swap(a1,b1,false,false);
      u32x4 ov={r0[0],r1[0],r0[1],r1[1]};
      const int tok=rt*256+64*wr+16*m+i16;
      st_sc1(rO,(unsigned)(tok*2048+(ct*2+wh)*64+offg*2),ov);
    }
  }
  STAMP(6);
}
}

namespace mix1 {
constexpr int HPART=64*256, WPART=64*448, SLOT=HPART+WPART;
constexpr int BIAS_OFF=3*SLOT;
constexpr int PFD_OFF=BIAS_OFF+1024;
constexpr int LDS_BYTES=PFD_OFF+256;
struct Ctx { unsigned bH[4], bW[2], vH[3], vW[4]; i32x4 srdH, srdW; unsigned mH, mH3, mW, soffH, soffW, wid; Pf pf, pf2; unsigned long long* its; };
DI void dma_w01(unsigned wid, unsigned voff, const i32x4& srd, unsigned soff){
  asm volatile("s_cmp_gt_u32 %3, 1\n\ts_cbranch_scc1 .Lsk%=\n\tbuffer_load_dwordx4 %0, %1, %2 offen lds\n.Lsk%=:" :: "v"(voff), "s"(srd), "s"(soff), "s"(wid) : "memory", "scc"); }
template<bool ISPF,int SET,int SL,bool READ,bool DMA,int VM>
DI void iter(Ctx& c, f32x4 (&acc)[7][4], u32x4 (&fH)[2][4], u32x4 (&fW)[2][7]){
  constexpr int NX=SET^1;
  constexpr int RD=((SL+1)%3==2)? -2*SLOT : SLOT;
  if constexpr(VM>=0){ if constexpr(!ISPF) waitv<VM>(); bar(); }
  u32x2 hl[4],hh[4],wl[7],wh[7];
  constexpr bool DO=DMA && !ISPF, DP=DMA && ISPF;
  auto RH=[&](auto ic,auto hc){ constexpr int i=decltype(ic)::value,h=decltype(hc)::value;
    if constexpr(READ){ if constexpr(h==0) ds_tr<0>(hl[i],c.bH[i]); else { ds_tr<1024>(hh[i],c.bH[i]); fH[NX][i]=cat(hl[i],hh[i]); vaddi<RD>(c.bH[i]); } } };
  auto RW=[&](auto ic,auto hc){ constexpr int i=decltype(ic)::value,h=decltype(hc)::value; constexpr int off=HPART+(i&~1)*32;
    if constexpr(READ){ if constexpr(h==0) ds_tr<off>(wl[i],c.bW[i&1]); else { ds_tr<off+4*448>(wh[i],c.bW[i&1]); fW[NX][i]=cat(wl[i],wh[i]); if constexpr(i>=5) vaddi<RD>(c.bW[i&1]); } } };
  auto MH=[&](){ if constexpr(DO) set_m0<SL*SLOT>(c.mH); };
  auto MH3=[&](){ if constexpr(DO) set_m0<SL*SLOT>(c.mH3); };
  auto MW=[&](){ if constexpr(DO) set_m0<SL*SLOT>(c.mW); if constexpr(DP) pf_m0(c.pf); };
  auto DH=[&](auto kc){ constexpr int k=decltype(kc)::value; if constexpr(DO){ if constexpr(k<2) dma<k*1024>(c.vH[k],c.srdH,c.soffH); else dma_w01(c.wid,c.vH[2],c.srdH,c.soffH); } };
  auto DW=[&](auto kc){ constexpr int k=decltype(kc)::value; if constexpr(DO) dma<k*1024>(c.vW[k],c.srdW,c.soffW);

                        };
  auto MM=[&](auto ac,auto bc){ constexpr int a=decltype(ac)::value,b=decltype(bc)::value; mfma(acc[a][b],fW[SET][a],fH[SET][b]); };
  MM(IC(0),IC(0)); RH(IC(0),IC(0)); MW();
  MM(IC(0),IC(1)); RH(IC(0),IC(1));
  MM(IC(0),IC(2)); RW(IC(0),IC(0)); DW(IC(0));
  MM(IC(0),IC(3)); RW(IC(0),IC(1));
  MM(IC(1),IC(0)); RH(IC(1),IC(0));
  MM(IC(1),IC(1)); RH(IC(1),IC(1)); DW(IC(1));
  MM(IC(1),IC(2)); RW(IC(1),IC(0));
  MM(IC(1),IC(3)); RW(IC(1),IC(1));
  MM(IC(2),IC(0)); RH(IC(2),IC(0));
  MM(IC(2),IC(1)); RH(IC(2),IC(1)); DW(IC(2));
  MM(IC(2),IC(2)); RW(IC(2),IC(0));
  MM(IC(2),IC(3)); RW(IC(2),IC(1));
  MM(IC(3),IC(0)); RH(IC(3),IC(0));
  MM(IC(3),IC(1)); RH(IC(3),IC(1)); DW(IC(3));
  MM(IC(3),IC(2)); RW(IC(3),IC(0)); MH();
  MM(IC(3),IC(3)); RW(IC(3),IC(1));
  MM(IC(4),IC(0)); RW(IC(4),IC(0)); DH(IC(0));
  MM(IC(4),IC(1)); RW(IC(4),IC(1));
  MM(IC(4),IC(2)); RW(IC(5),IC(0));
  MM(IC(4),IC(3)); RW(IC(5),IC(1)); DH(IC(1));
  MM(IC(5),IC(0)); RW(IC(6),IC(0)); MH3();
  MM(IC(5),IC(1)); RW(IC(6),IC(1));
  MM(IC(5),IC(2)); DH(IC(2));
  if constexpr(DO){ saddi<64*2048>(c.soffH); saddi<64*7168>(c.soffW); }
  MM(IC(5),IC(3));
  MM(IC(6),IC(0)); MM(IC(6),IC(1)); MM(IC(6),IC(2));
  if constexpr(DP){ pf_touch(c.pf); pf_touch2(c.pf2); }
  MM(IC(6),IC(3));
  if constexpr(READ) waitl<0>();
}
DI float gelu1(float x){
  const float k1=-2.0f*0.7978845608028654f*1.4426950408889634f, k2=k1*0.044715f;
  const float t=x*__builtin_fmaf(x*x,k2,k1);
  return x*__builtin_amdgcn_rcpf(1.0f+__builtin_amdgcn_exp2f(t));
}
template<int KG>
DI void epilogue(const Params& p, LAS char* sm, f32x4 (&acc)[7][4], int w, int d0, int f0){
  const int tid=otid(), l=tid&63, dh=w&1, fh=(w>>1)&1;
  const int g=l>>4, i16=l&15;
  { LAS char* wb=sm+w*14336+l*16;
    #pragma unroll
    for(int a=0;a<7;a++){ *(LAS f32x4*)(wb+(a*2+0)*1024)=acc[a][2*(1-KG)]; *(LAS f32x4*)(wb+(a*2+1)*1024)=acc[a][2*(1-KG)+1]; } }
  u32x2 bb[7];
  #pragma unroll
  for(int a=0;a<7;a++) bb[a]=*(LAS u32x2*)(sm+BIAS_OFF+((fh*7+a)*16+4*g)*2);
  __syncthreads();
  u32x2 res[7][2];
  { LAS char* rb=sm+(w^4)*14336+l*16;
    #pragma unroll
    for(int a=0;a<7;a++){
      const float b0=bflo(bb[a][0]),b1=bfhi(bb[a][0]),b2=bflo(bb[a][1]),b3=bfhi(bb[a][1]);
      #pragma unroll
      for(int b=0;b<2;b++){
        const f32x4 x=acc[a][2*KG+b]+*(LAS f32x4*)(rb+(a*2+b)*1024);
        res[a][b][0]=cvtpk(gelu1(x[0]+b0),gelu1(x[1]+b1)); res[a][b][1]=cvtpk(gelu1(x[2]+b2),gelu1(x[3]+b3));
      } } }
  __syncthreads();
  constexpr int RSO=464;
  #pragma unroll
  for(int a=0;a<7;a++)
  #pragma unroll
  for(int b=0;b<2;b++) *(LAS u32x2*)(sm+(64*dh+16*(2*KG+b)+i16)*RSO+((fh*7+a)*16+4*g)*2)=res[a][b];
  __syncthreads();
  const __amdgpu_buffer_rsrc_t rU=make_rsrc(p.ws+WS_U,D*F*2);
  #pragma unroll
  for(int k=0;k<7;k++){ const int n=k*512+tid; const int row=n/28, ch=n-row*28; const u32x4 x=*(LAS u32x4*)(sm+row*RSO+ch*16); st_sc1(rU,(unsigned)((d0+row)*7168+f0*2+ch*16),x); }
}
template<bool ISPF>
DI void run(const Params& p, char* smem, int dt, int ft, unsigned long long* stamps){
  const unsigned lds0=(unsigned)(size_t)(LAS char*)smem;
  const int tid=threadIdx.x, w=ISPF?7:rfl(tid>>6), l=tid&63;
  const int g=l>>4,q=(l&15)>>2,pp=l&3;
  const int dh=w&1, fh=(w>>1)&1, kg=w>>2;
  const int d0=dt*128, f0=ft*224;
  Ctx c; f32x4 acc[7][4]; u32x4 fH[2][4], fW[2][7];
  c.its=stamps+8192+blockIdx.x*64;
  if constexpr(!ISPF){
    c.srdH=make_srd(p.ws+WS_H,S*D*2); c.srdW=make_srd(p.w1,S*F*2); c.wid=(unsigned)w;
    c.mH=rfl(lds0+w*2048); c.mH3=rfl(lds0+(14+(w&1))*1024); c.mW=rfl(lds0+HPART+w*4096);
    #pragma unroll
    for(int k=0;k<3;k++){ const int j=(k<2)? 2*w+k : 14+(w&1); const int row=4*j+(l>>4); const int sw=(row&3)|(((row>>3)&1)<<2); const int pos=l&15; const int ch=(((pos>>1)^sw)<<1)|(pos&1);
      c.vH[k]=row*2048+d0*2+ch*16-((k<2)?1024*k:0); }
    #pragma unroll
    for(int k=0;k<4;k++){ const int n=64*(4*w+k)+l; const int row=n/28,pos=n-row*28; const int ch=pos^(((row>>3)&1)<<1); c.vW[k]=row*7168+f0*2+ch*16-1024*k; }
  } else {
    c.pf.srd=make_srd(p.w1,S*F*2); c.pf.voff=(l<32)? (unsigned)((8*dt+(l>>2))*7168+f0*2+(l&3)*128) : 0x80000000u; c.pf.soff=3*64*7168; c.pf.step=64*7168; c.pf.m0=rfl(lds0+PFD_OFF);
    c.pf2.srd=make_srd(p.w2,F*S*2); c.pf2.voff=(l<32)? (unsigned)(((ft*8+dt)*32+l)*128) : 0x80000000u; c.pf2.soff=0; c.pf2.step=128*32*128; c.pf2.m0=c.pf.m0;
  }
  { const int row=32*kg+8*g+q; const int sw=q|((g&1)<<2);
    #pragma unroll
    for(int e=0;e<4;e++) c.bH[e]=lds0+row*256+(((4*dh+e)^sw)*32)+8*pp;
    const int b3=g&1;
    #pragma unroll
    for(int e=0;e<2;e++) c.bW[e]=lds0+row*448+(((fh*7+e)^b3)*32)+8*pp; }
  #pragma unroll
  for(int a=0;a<7;a++)
  #pragma unroll
  for(int b=0;b<4;b++){ acc[a][b]=(f32x4){0,0,0,0}; pin(acc[a][b]); }
  STAMP(1);
  if constexpr(!ISPF){
    c.soffH=0; c.soffW=0;
    auto tile=[&](auto tc){ constexpr int T=decltype(tc)::value;
      set_m0<T*SLOT>(c.mW); nop1(); dma<0>(c.vW[0],c.srdW,c.soffW); dma<1024>(c.vW[1],c.srdW,c.soffW); dma<2048>(c.vW[2],c.srdW,c.soffW); dma<3072>(c.vW[3],c.srdW,c.soffW);
      set_m0<T*SLOT>(c.mH); nop1(); dma<0>(c.vH[0],c.srdH,c.soffH); dma<1024>(c.vH[1],c.srdH,c.soffH);
      set_m0<T*SLOT>(c.mH3); nop1(); dma_w01(c.wid,c.vH[2],c.srdH,c.soffH);
      saddi<64*2048>(c.soffH); saddi<64*7168>(c.soffW); };
    tile(IC(0)); tile(IC(1)); tile(IC(2));
    waitv<12>();
  } else {
    i32x4 srdB=make_srd(p.b1,F*2); set_m0<0>(rfl(lds0+BIAS_OFF)); nop1(); dma<0>((unsigned)(f0*2+l*16), srdB, 0u);
    pf_m0(c.pf); nop1();
    #pragma unroll
    for(int k=0;k<PFM0;k++) pf_touch(c.pf);
  }
  bar();
  { auto rd2=[&](auto o0,auto o1,u32x4& f,unsigned a){ u32x2 lo,hi; ds_tr<decltype(o0)::value>(lo,a); ds_tr<decltype(o1)::value>(hi,a); f=cat(lo,hi); };
    rd2(IC(0),IC(1024),fH[0][0],c.bH[0]); rd2(IC(0),IC(1024),fH[0][1],c.bH[1]); rd2(IC(0),IC(1024),fH[0][2],c.bH[2]); rd2(IC(0),IC(1024),fH[0][3],c.bH[3]);
    rd2(IC(HPART),IC(HPART+4*448),fW[0][0],c.bW[0]); rd2(IC(HPART),IC(HPART+4*448),fW[0][1],c.bW[1]);
    rd2(IC(HPART+64),IC(HPART+64+4*448),fW[0][2],c.bW[0]); rd2(IC(HPART+64),IC(HPART+64+4*448),fW[0][3],c.bW[1]);
    rd2(IC(HPART+128),IC(HPART+128+4*448),fW[0][4],c.bW[0]); rd2(IC(HPART+128),IC(HPART+128+4*448),fW[0][5],c.bW[1]);
    rd2(IC(HPART+192),IC(HPART+192+4*448),fW[0][6],c.bW[0]);
    vaddi<SLOT>(c.bH[0]); vaddi<SLOT>(c.bH[1]); vaddi<SLOT>(c.bH[2]); vaddi<SLOT>(c.bH[3]); vaddi<SLOT>(c.bW[0]); vaddi<SLOT>(c.bW[1]);
    waitl<0>(); }
  STAMP(2);
  { constexpr int ND=6;
    for(int t=0;t<24;t+=6){
      iter<ISPF,0,0,true,true,ND>(c,acc,fH,fW); iter<ISPF,1,1,true,true,ND>(c,acc,fH,fW); iter<ISPF,0,2,true,true,ND>(c,acc,fH,fW);
      iter<ISPF,1,0,true,true,ND>(c,acc,fH,fW); iter<ISPF,0,1,true,true,ND>(c,acc,fH,fW); iter<ISPF,1,2,true,true,ND>(c,acc,fH,fW); }
    iter<ISPF,0,0,true,true,ND>(c,acc,fH,fW); iter<ISPF,1,1,true,true,ND>(c,acc,fH,fW); iter<ISPF,0,2,true,true,ND>(c,acc,fH,fW);
    iter<ISPF,1,0,true,true,ND>(c,acc,fH,fW); iter<ISPF,0,1,true,true,ND>(c,acc,fH,fW);
    iter<ISPF,1,2,true,false,ND>(c,acc,fH,fW);
    iter<ISPF,0,0,true,false,0>(c,acc,fH,fW);
    iter<ISPF,1,1,false,false,-1>(c,acc,fH,fW); }
  asm volatile("s_nop 7\n\ts_nop 7" ::: "memory");
  #pragma unroll
  for(int a=0;a<7;a++)
  #pragma unroll
  for(int b=0;b<4;b++) pin(acc[a][b]);
  STAMP(3);
  if constexpr(ISPF) waitv<0>();
  bar();
  if(w<4) epilogue<0>(p,(LAS char*)smem,acc,w,d0,f0); else epilogue<1>(p,(LAS char*)smem,acc,w,d0,f0);
  STAMP(4);
}
}

__global__ __launch_bounds__(512,2) void k_phaseA(Params p){
  extern __shared__ __attribute__((aligned(16))) char smem[];
  unsigned long long* stamps=(unsigned long long*)(p.ws+WS_STM);
  (void)stamps;
  RSTAMP(0);
  const int b=blockIdx.x, xcd=b&7, j=b>>3;
  const bool pfw = rfl(threadIdx.x>>6)==7;
  if(xcd<4){ if(pfw) qkv::run<true>(p,smem,j&7,xcd*4+(j>>3),1u,stamps); else qkv::run<false>(p,smem,j&7,xcd*4+(j>>3),1u,stamps); }
  else { if(pfw) mix1::run<true>(p,smem,j&7,(xcd-4)*4+(j>>3),stamps); else mix1::run<false>(p,smem,j&7,(xcd-4)*4+(j>>3),stamps); }
  RSTAMP(15);
}

namespace fin {
constexpr int PART=16384, SLOT=2*PART, NS=4, GB_OFF=NS*SLOT, LDS_BYTES=GB_OFF+2048;
struct Src { i32x4 srd; unsigned v[2]; unsigned soff; };
struct Bases;
template<int SET,int SL,bool READ,bool RPA,int DMAK  ,int VM,class BT>
DI void iter(const BT& RB, unsigned mS, unsigned mD, Src& sS, Src& sD, f32x4 (&acc)[4][4], u32x4 (&fS)[2][4], u32x4 (&fD)[2][4]){
  constexpr int NX=SET^1, RS=(SL+1)&3, DS=(SL+3)&3;
  constexpr int RHI=RS>>1, RO=(RS&1)*SLOT;
  if constexpr(VM>=0){ waitv<VM>(); bar(); }
  u32x2 tl[4],th[4];
  auto RR=[&](auto ic){ constexpr int i=decltype(ic)::value;
    if constexpr(READ){ if constexpr(RPA) ds_b128<RO+i*2048>(fS[NX][i],RB.aR[RHI]); else ds_b128<RO+PART+i*2048>(fD[NX][i],RB.aR[RHI]); } };
  auto RT=[&](auto ic,auto hc){ constexpr int i=decltype(ic)::value,h=decltype(hc)::value;
    if constexpr(READ){
      if constexpr(RPA){ if constexpr(h==0) ds_tr<RO+PART>(tl[i],RB.aT[RHI][i]); else { ds_tr<RO+PART+1024>(th[i],RB.aT[RHI][i]); fD[NX][i]=cat(tl[i],th[i]); } }
      else { if constexpr(h==0) ds_tr<RO>(tl[i],RB.aT[RHI][i]); else { ds_tr<RO+1024>(th[i],RB.aT[RHI][i]); fS[NX][i]=cat(tl[i],th[i]); } } } };
  auto MS=[&](){ if constexpr(DMAK!=0) set_m0<DS*SLOT>(mS); };
  auto MD=[&](){ if constexpr(DMAK!=0) set_m0<DS*SLOT>(mD); };
  auto DSs=[&](auto kc){ constexpr int k=decltype(kc)::value; if constexpr(DMAK!=0) dma<k*1024>(sS.v[k],sS.srd,sS.soff); };
  auto DDd=[&](auto kc){ constexpr int k=decltype(kc)::value; if constexpr(DMAK!=0) dma<k*1024>(sD.v[k],sD.srd,sD.soff); };
  auto MM=[&](auto ac,auto bc){ constexpr int a=decltype(ac)::value,b=decltype(bc)::value; mfma(acc[a][b],fD[SET][a],fS[SET][b]); };
  MM(IC(0),IC(0)); RR(IC(0)); MS();
  MM(IC(0),IC(1)); RT(IC(0),IC(0)); DSs(IC(0));
  MM(IC(0),IC(2)); RT(IC(0),IC(1));
  MM(IC(0),IC(3)); RR(IC(1)); DSs(IC(1));
  MM(IC(1),IC(0)); RT(IC(1),IC(0)); MD();
  MM(IC(1),IC(1)); RT(IC(1),IC(1)); DDd(IC(0));
  MM(IC(1),IC(2)); RR(IC(2));
  MM(IC(1),IC(3)); RT(IC(2),IC(0)); DDd(IC(1));
  MM(IC(2),IC(0)); RT(IC(2),IC(1));
  MM(IC(2),IC(1)); RR(IC(3));
  if constexpr(DMAK==1){ saddi<128>(sS.soff); saddi<64*2048>(sD.soff); }
  if constexpr(DMAK==2){ saddi<64*4096>(sS.soff); saddi<128>(sD.soff); }
  MM(IC(2),IC(2)); RT(IC(3),IC(0));
  MM(IC(2),IC(3)); RT(IC(3),IC(1));
  MM(IC(3),IC(0)); MM(IC(3),IC(1)); MM(IC(3),IC(2)); MM(IC(3),IC(3));
  if constexpr(READ) waitl<0>();
}
struct Bases { unsigned aR[2], aT[2][4]; };
template<bool PA> DI void mk_bases(Bases& B, unsigned lds0, int wsd, int wd, int kg){
  const int l=otid()&63, g=l>>4,q=(l&15)>>2,pp=l&3,i16=l&15;
  #pragma unroll
  for(int hi=0;hi<2;hi++){
    const unsigned base=lds0+hi*2*SLOT;
    B.aR[hi]=base+(64*(PA?wsd:wd)+i16)*128+(((4*kg+g)^((i16>>1)&7))*16);
    const int row=32*kg+8*g+q; const int sw=q|((g&1)<<2);
    #pragma unroll
    for(int e=0;e<4;e++) B.aT[hi][e]=base+row*256+(((4*(PA?wd:wsd)+e)^sw)*32)+8*pp;
  }
}
template<bool PA> DI void mk_src(const Params& p, Src& sS, Src& sD, int w, int s0, int d0, int half){
  const int l=otid()&63;
  #pragma unroll
  for(int k=0;k<2;k++){
    const int rowR=16*w+8*k+(l>>3); const int chR=(l&7)^((rowR>>1)&7);
    const int rowT=8*w+4*k+(l>>4); const int sw=(rowT&3)|(((rowT>>3)&1)<<2); const int pos=l&15; const int chT=(((pos>>1)^sw)<<1)|(pos&1);
    if constexpr(PA){ sS.v[k]=(s0+rowR)*2048+chR*16-1024*k; sD.v[k]=rowT*2048+d0*2+chT*16-1024*k; }
    else            { sS.v[k]=rowT*4096+s0*2+chT*16-1024*k; sD.v[k]=(d0+rowR)*7168+chR*16-1024*k; }
  }
  if constexpr(PA){ sS.srd=make_srd(p.ws+WS_O,S*D*2); sD.srd=make_srd(p.wo,D*D*2); sS.soff=0; sD.soff=0; }
  else { sS.srd=make_srd(p.w2,F*S*2); sD.srd=make_srd(p.ws+WS_U,D*F*2); sS.soff=rfl(half? 20*64*4096 : 0); sD.soff=rfl(half? 20*128 : 0); }
}
DI void run(const Params& p, char* smem, int st, int dt, int half, unsigned epoch, unsigned long long* stamps){
  const unsigned lds0=(unsigned)(size_t)(LAS char*)smem;
  LAS char* sm=(LAS char*)smem;
  const int w=rfl(threadIdx.x>>6);
  const int wsd=w&1, wd=(w>>1)&1, kg=w>>2;
  const int s0=st*128, d0=dt*128;
  const unsigned mS=rfl(lds0+w*2048), mD=rfl(lds0+PART+w*2048);
  f32x4 accA[4][4], accB[4][4]; u32x4 fS[2][4], fD[2][4];
  #pragma unroll
  for(int a=0;a<4;a++)
  #pragma unroll
  for(int b=0;b<4;b++){ accA[a][b]=(f32x4){0,0,0,0}; accB[a][b]=(f32x4){0,0,0,0}; pin(accA[a][b]); pin(accB[a][b]); }
  STAMP(1);
  if(w==0){ const int l=threadIdx.x&63; i32x4 sg=make_srd(p.ws+WS_G,S*4); set_m0<0>(rfl(lds0+GB_OFF)); nop1(); dma<0>((unsigned)(s0*4+l*16),sg,0u); }
  if(w==1){ const int l=threadIdx.x&63; i32x4 sb=make_srd(p.b2,S*2); set_m0<0>(rfl(lds0+GB_OFF+1024)); nop1(); dma<0>((unsigned)(s0*2+l*16),sb,0u); }
  auto tile=[&](auto slc, auto pac, Src& sS, Src& sD){ constexpr int SLT=decltype(slc)::value; constexpr bool PA=decltype(pac)::value!=0;
    set_m0<SLT*SLOT>(mS); nop1(); dma<0>(sS.v[0],sS.srd,sS.soff); dma<1024>(sS.v[1],sS.srd,sS.soff);
    set_m0<SLT*SLOT>(mD); nop1(); dma<0>(sD.v[0],sD.srd,sD.soff); dma<1024>(sD.v[1],sD.srd,sD.soff);
    if constexpr(PA){ saddi<128>(sS.soff); saddi<64*2048>(sD.soff); } else { saddi<64*4096>(sS.soff); saddi<128>(sD.soff); } };
  auto first_read=[&](auto pac, const Bases& B){ constexpr bool PA=decltype(pac)::value!=0;
    u32x2 l0,h0,l1,h1,l2,h2,l3,h3;
    if constexpr(PA){
      ds_b128<0>(fS[0][0],B.aR[0]); ds_b128<2048>(fS[0][1],B.aR[0]); ds_b128<4096>(fS[0][2],B.aR[0]); ds_b128<6144>(fS[0][3],B.aR[0]);
      ds_tr<PART>(l0,B.aT[0][0]); ds_tr<PART+1024>(h0,B.aT[0][0]); ds_tr<PART>(l1,B.aT[0][1]); ds_tr<PART+1024>(h1,B.aT[0][1]);
      ds_tr<PART>(l2,B.aT[0][2]); ds_tr<PART+1024>(h2,B.aT[0][2]); ds_tr<PART>(l3,B.aT[0][3]); ds_tr<PART+1024>(h3,B.aT[0][3]);
      fD[0][0]=cat(l0,h0); fD[0][1]=cat(l1,h1); fD[0][2]=cat(l2,h2); fD[0][3]=cat(l3,h3);
    } else {
      ds_b128<PART>(fD[0][0],B.aR[0]); ds_b128<PART+2048>(fD[0][1],B.aR[0]); ds_b128<PART+4096>(fD[0][2],B.aR[0]); ds_b128<PART+6144>(fD[0][3],B.aR[0]);
      ds_tr<0>(l0,B.aT[0][0]); ds_tr<1024>(h0,B.aT[0][0]); ds_tr<0>(l1,B.aT[0][1]); ds_tr<1024>(h1,B.aT[0][1]);
      ds_tr<0>(l2,B.aT[0][2]); ds_tr<1024>(h2,B.aT[0][2]); ds_tr<0>(l3,B.aT[0][3]); ds_tr<1024>(h3,B.aT[0][3]);
      fS[0][0]=cat(l0,h0); fS[0][1]=cat(l1,h1); fS[0][2]=cat(l2,h2); fS[0][3]=cat(l3,h3);
    }
    waitl<0>(); };
  #define ITA(SET,SL,RPA,DK,RB,XS,XD) iter<SET,SL,true,RPA,DK,4>(RB,mS,mD,XS,XD,accA,fS,fD)
  #define ITB(SET,SL,RD,DK,VM) iter<SET,SL,RD,false,DK,VM>(Bb,mS,mD,bS,bD,accB,fS,fD)
  Bases Bb; Src bS,bD;
  if(half==0){
    Bases Ba; Src aS,aD;
    mk_src<true>(p,aS,aD,w,s0,d0,half); mk_bases<true>(Ba,lds0,wsd,wd,kg);
    tile(IC(0),IC(1),aS,aD); tile(IC(1),IC(1),aS,aD); tile(IC(2),IC(1),aS,aD);
    waitv<8>(); bar(); first_read(IC(1),Ba);
    STAMP(2);
    for(int t=0;t<12;t+=4){ ITA(0,0,true,1,Ba,aS,aD); ITA(1,1,true,1,Ba,aS,aD); ITA(0,2,true,1,Ba,aS,aD); ITA(1,3,true,1,Ba,aS,aD); }
    ITA(0,0,true,1,Ba,aS,aD);
    mk_src<false>(p,bS,bD,w,s0,d0,half);
    ITA(1,1,true,2,Ba,bS,bD);
    ITA(0,2,true,2,Ba,bS,bD);
    mk_bases<false>(Bb,lds0,wsd,wd,kg);
    ITA(1,3,false,2,Bb,bS,bD);
  } else {
    mk_src<false>(p,bS,bD,w,s0,d0,half); mk_bases<false>(Bb,lds0,wsd,wd,kg);
    tile(IC(0),IC(0),bS,bD); tile(IC(1),IC(0),bS,bD); tile(IC(2),IC(0),bS,bD);
    waitv<8>(); bar(); first_read(IC(0),Bb);
    STAMP(2);
  }
  { const int n=half? 32 : 16;
    for(int t=0;t<n;t+=4){ ITB(0,0,true,2,4); ITB(1,1,true,2,4); ITB(0,2,true,2,4); ITB(1,3,true,2,4); } }
  ITB(0,0,true,2,4); ITB(1,1,true,0,4); ITB(0,2,true,0,0); ITB(1,3,false,0,-1);
  asm volatile("s_nop 7\n\ts_nop 7" ::: "memory");
  #pragma unroll
  for(int a=0;a<4;a++)
  #pragma unroll
  for(int b=0;b<4;b++){ pin(accA[a][b]); pin(accB[a][b]); }
  STAMP(3);
  const int tid=otid(), l=tid&63, g=l>>4, i16=l&15;
  float gt[4], bz[4];
  #pragma unroll
  for(int b=0;b<4;b++){ const int sl=64*wsd+16*b+i16; gt[b]=*(LAS float*)(sm+GB_OFF+sl*4); bz[b]=__uint_as_float(((unsigned)*(LAS u16*)(sm+GB_OFF+1024+sl*2))<<16); }
  #pragma unroll
  for(int a=0;a<4;a++)
  #pragma unroll
  for(int b=0;b<4;b++) accB[a][b]=accA[a][b]*gt[b]+accB[a][b]*(1.0f-gt[b]);
  bar();
  { LAS char* wb=sm+w*8192+l*16;
    if(kg==0){
      #pragma unroll
      for(int b=0;b<4;b++){ *(LAS f32x4*)(wb+(b*2+0)*1024)=accB[2][b]; *(LAS f32x4*)(wb+(b*2+1)*1024)=accB[3][b]; }
    } else {
      #pragma unroll
      for(int b=0;b<4;b++){ *(LAS f32x4*)(wb+(b*2+0)*1024)=accB[0][b]; *(LAS f32x4*)(wb+(b*2+1)*1024)=accB[1][b]; }
    } }
  __syncthreads();
  f32x4 r[2][4];
  { LAS char* rb=sm+(w^4)*8192+l*16;
    if(kg==0){
      #pragma unroll
      for(int b=0;b<4;b++){ r[0][b]=accB[0][b]+*(LAS f32x4*)(rb+(b*2+0)*1024); r[1][b]=accB[1][b]+*(LAS f32x4*)(rb+(b*2+1)*1024); }
    } else {
      #pragma unroll
      for(int b=0;b<4;b++){ r[0][b]=accB[2][b]+*(LAS f32x4*)(rb+(b*2+0)*1024); r[1][b]=accB[3][b]+*(LAS f32x4*)(rb+(b*2+1)*1024); }
    } }
  char* ws=p.ws;
  const int me=(st*8+dt)*2+half, other=me^1;
  const __amdgpu_buffer_rsrc_t rX=make_rsrc(ws+WS_XCH,256*32768);
  unsigned* flags=(unsigned*)(ws+WS_FLG);
  const int slot=(wd*2+kg)*8192+l*16;
  if(wsd!=half){
    #pragma unroll
    for(int b=0;b<4;b++){ st_sc1(rX,(unsigned)(me*32768+slot+(b*2+0)*1024),__builtin_bit_cast(u32x4,r[0][b])); st_sc1(rX,(unsigned)(me*32768+slot+(b*2+1)*1024),__builtin_bit_cast(u32x4,r[1][b])); }
  }
  asm volatile("s_waitcnt vmcnt(0)" ::: "memory");
  __syncthreads();
  if(tid==0) flag_st(flags+(FLG_X+me)*16, epoch);
  if(w==0) poll_flags(flags, FLG_X+other, 1, epoch, l);
  __syncthreads();
  STAMP(4);
  if(wsd==half){
    u32x4 t[2][4];
    #pragma unroll
    for(int b=0;b<4;b++){ t[0][b]=ld_sc1(rX,(unsigned)(other*32768+slot+(b*2+0)*1024)); t[1][b]=ld_sc1(rX,(unsigned)(other*32768+slot+(b*2+1)*1024)); }
    const int offg=(g&1)*16+(g>>1)*8;
    #pragma unroll
    for(int b=0;b<4;b++){
      const float add=(1.0f-gt[b])*bz[b];
      const f32x4 x0=r[0][b]+__builtin_bit_cast(f32x4,t[0][b])+add, x1=r[1][b]+__builtin_bit_cast(f32x4,t[1][b])+add;
      unsigned a0=cvtpk(x0[0],x0[1]), a1=cvtpk(x0[2],x0[3]), b0=cvtpk(x1[0],x1[1]), b1=cvtpk(x1[2],x1[3]);
      auto r0=__builtin_amdgcn_permlane16_swap(a0,b0,false,false); auto r1=__builtin_amdgcn_permlane16_swap(a1,b1,false,false);
      const u32x4 ov={r0[0],r1[0],r0[1],r1[1]};
      const int s=s0+64*wsd+16*b+i16;
      *(u32x4*)(p.out+(size_t)s*D+d0+64*wd+32*kg+offg)=ov;
    }
  }
  STAMP(5);
}
}

__global__ __launch_bounds__(512,2) void k_final(Params p){
  extern __shared__ __attribute__((aligned(16))) char smem[];
  unsigned long long* stamps=(unsigned long long*)(p.ws+WS_STM)+4096;
  (void)stamps;
  RSTAMP(0);
  const int b=blockIdx.x, xcd=b&7, j=b>>3;
  const int half=xcd&1, grp=xcd>>1;
  const int st=(grp&1)*8+(j&7), dt=(grp>>1)*4+(j>>3);
  fin::run(p,smem,st,dt,half,1u,stamps);
  RSTAMP(15);
}

extern "C" void kernel_launch(void* const* d_in, const int* in_sizes, int n_in, void* d_out, int out_size, void* d_ws, size_t ws_size, hipStream_t stream){
  (void)in_sizes; (void)n_in; (void)out_size; (void)ws_size;
  Params p;
  p.x=(const u16*)d_in[0]; p.wqkv=(const u16*)d_in[1]; p.wphi=(const u16*)d_in[2]; p.wo=(const u16*)d_in[3];
  p.w1=(const u16*)d_in[5]; p.b1=(const u16*)d_in[6]; p.w2=(const u16*)d_in[7]; p.b2=(const u16*)d_in[8]; p.gw=(const u16*)d_in[9];
  p.out=(u16*)d_out; p.ws=(char*)d_ws;
  constexpr int LDS_A = (qkv::LDS_BYTES>mix1::LDS_BYTES)?qkv::LDS_BYTES:mix1::LDS_BYTES;
  (void)hipFuncSetAttribute((const void*)k_phaseA, hipFuncAttributeMaxDynamicSharedMemorySize, LDS_A);
  (void)hipFuncSetAttribute((const void*)k_final, hipFuncAttributeMaxDynamicSharedMemorySize, fin::LDS_BYTES);
  k_ln<<<256,512,0,stream>>>(p);
  k_phaseA<<<256,512,LDS_A,stream>>>(p);
  k_final<<<256,512,fin::LDS_BYTES,stream>>>(p);
}
```

```cpp
#include <hip/hip_runtime.h>
#include <hip/hip_bf16.h>
#include <type_traits>
#include <utility>

#define LAS __attribute__((address_space(3)))
#define DI __device__ __forceinline__
#define IC(n) std::integral_constant<int,(n)>{}
typedef unsigned short u16;
typedef float f32x4 __attribute__((ext_vector_type(4)));
typedef float f32x2 __attribute__((ext_vector_type(2)));
typedef int i32x4 __attribute__((ext_vector_type(4)));
typedef unsigned u32x4 __attribute__((ext_vector_type(4)));
typedef unsigned u32x2 __attribute__((ext_vector_type(2)));
typedef short bf16x8 __attribute__((ext_vector_type(8)));
typedef __bf16 bf16x2_t __attribute__((ext_vector_type(2)));

constexpr int S=2048, D=1024, F=3584;
constexpr size_t WS_H   = 0;
constexpr size_t WS_U   = WS_H + (size_t)S*D*2;
constexpr size_t WS_O   = WS_U + (size_t)D*F*2;
constexpr size_t WS_KVP = WS_O + (size_t)S*D*2;
constexpr size_t WS_ZP  = WS_KVP + (size_t)16*8*8*1024;
constexpr size_t WS_G   = WS_ZP + (size_t)16*8*2*64*4;
constexpr size_t WS_XCH = WS_G + 8192;
constexpr size_t WS_FLG = WS_XCH + (size_t)128*65536;
constexpr size_t WS_STM = WS_FLG + 131072;
#define FST 32
constexpr int FLG_LN = 64;
constexpr int FLG_KV = 192;
constexpr int FLG_O  = 320;
constexpr int FLG_U  = 448;
constexpr int FLG_X  = 576;

template<int OFF> DI void ds_tr(u32x2& d, unsigned a){ asm volatile("ds_read_b64_tr_b16 %0, %1 offset:%2" : "=v"(d) : "v"(a), "i"(OFF)); }
template<int OFF> DI void ds_b128(u32x4& d, unsigned a){ asm volatile("ds_read_b128 %0, %1 offset:%2" : "=v"(d) : "v"(a), "i"(OFF)); }
DI void mfma(f32x4& c, const u32x4& a, const u32x4& b){ asm volatile("v_mfma_f32_16x16x32_bf16 %0, %1, %2, %0" : "+v"(c) : "v"(a), "v"(b)); }
DI f32x4 mfma_b(const u32x4& a, const u32x4& b, const f32x4& c){ return __builtin_amdgcn_mfma_f32_16x16x32_bf16(__builtin_bit_cast(bf16x8,a), __builtin_bit_cast(bf16x8,b), c, 0,0,0); }
template<int IMM,int POL=0> DI void dma(unsigned voff, const i32x4& srd, unsigned soff){
  if constexpr(POL==2) asm volatile("buffer_load_dwordx4 %0, %1, %2 offen offset:%3 sc1 lds" :: "v"(voff), "s"(srd), "s"(soff), "i"(IMM) : "memory");
  else if constexpr(POL==1) asm volatile("buffer_load_dwordx4 %0, %1, %2 offen offset:%3 nt lds" :: "v"(voff), "s"(srd), "s"(soff), "i"(IMM) : "memory");
  else asm volatile("buffer_load_dwordx4 %0, %1, %2 offen offset:%3 lds" :: "v"(voff), "s"(srd), "s"(soff), "i"(IMM) : "memory"); }
template<int IMM> DI void touch(unsigned& d, unsigned voff, const i32x4& srd, unsigned soff){
  asm volatile("buffer_load_dword %0, %1, %2, %3 offen offset:%4" : "+v"(d) : "v"(voff), "s"(srd), "s"(soff), "i"(IMM) : "memory"); }
template<int V> DI void set_m0(unsigned base){ asm volatile("s_add_u32 m0, %0, %1" :: "s"(base), "i"(V) : "scc"); }
DI void nop1(){ asm volatile("s_nop 0"); }
template<int N> DI void waitv(){ asm volatile("s_waitcnt vmcnt(%0)" :: "n"(N) : "memory"); }
template<int N> DI void waitl(){ asm volatile("s_waitcnt lgkmcnt(%0)" :: "n"(N) : "memory"); }
DI void bar(){ asm volatile("s_barrier" ::: "memory"); }
template<int V> DI void saddi(unsigned& x){ asm volatile("s_add_u32 %0, %0, %1" : "+s"(x) : "i"(V) : "scc"); }
DI void pin(f32x4& x){ asm volatile("" : "+v"(x)); }
DI void pin(u32x4& x){ asm volatile("" : "+v"(x)); }
DI void pin(u32x2& x){ asm volatile("" : "+v"(x)); }
DI void pin(unsigned& x){ asm volatile("" : "+v"(x)); }
DI u32x4 cat(u32x2 lo,u32x2 hi){ return __builtin_shufflevector(lo,hi,0,1,2,3); }
DI unsigned cvtpk(float lo,float hi){ f32x2 v={lo,hi}; bf16x2_t b=__builtin_convertvector(v,bf16x2_t); return __builtin_bit_cast(unsigned,b); }
DI unsigned cvtpk(f32x2 v){ bf16x2_t b=__builtin_convertvector(v,bf16x2_t); return __builtin_bit_cast(unsigned,b); }
DI float bflo(unsigned x){ return __uint_as_float(x<<16); }
DI float bfhi(unsigned x){ return __uint_as_float(x&0xffff0000u); }
DI f32x2 bf2(unsigned x){ return (f32x2){bflo(x),bfhi(x)}; }
DI f32x2 lo2(const f32x4& v){ return __builtin_shufflevector(v,v,0,1); }
DI f32x2 hi2(const f32x4& v){ return __builtin_shufflevector(v,v,2,3); }
DI f32x2 fma2(f32x2 a,f32x2 b,f32x2 c){ return __builtin_elementwise_fma(a,b,c); }
DI f32x2 bc2(float x){ return (f32x2){x,x}; }
DI int otid(){ int t=threadIdx.x; asm volatile("" : "+v"(t)); return t; }
DI unsigned rfl(unsigned x){ return __builtin_amdgcn_readfirstlane(x); }
DI i32x4 make_srd(const void* p, unsigned bytes){
  unsigned long long a=(unsigned long long)p; i32x4 s;
  s[0]=(int)rfl((unsigned)a); s[1]=(int)(rfl((unsigned)(a>>32))&0xffff); s[2]=(int)bytes; s[3]=0x00020000; return s; }
DI __amdgpu_buffer_rsrc_t make_rsrc(const void* p, unsigned bytes){ return __builtin_amdgcn_make_buffer_rsrc((void*)p, 0, bytes, 0x00020000); }
DI void st_sc1(__amdgpu_buffer_rsrc_t r, unsigned off, const u32x4& v){ __builtin_amdgcn_raw_buffer_store_b128(v, r, off, 0, 16); }
DI u32x4 ld_sc1(__amdgpu_buffer_rsrc_t r, unsigned off){ return __builtin_amdgcn_raw_buffer_load_b128(r, off, 0, 16); }
template<int OFF> DI void gld128(u32x4& d, const void* p){ asm volatile("global_load_dwordx4 %0, %1, off offset:%2" : "=v"(d) : "v"(p), "i"(OFF) : "memory"); }
template<int OFF> DI void gld64(u32x2& d, const void* p){ asm volatile("global_load_dwordx2 %0, %1, off offset:%2" : "=v"(d) : "v"(p), "i"(OFF) : "memory"); }
DI void gld32_sc1(unsigned& d, const void* p){ asm volatile("global_load_dword %0, %1, off sc1" : "=v"(d) : "v"(p) : "memory"); }
typedef unsigned pr2 __attribute__((ext_vector_type(2)));
DI pr2 sw16(unsigned a,unsigned b){ auto r=__builtin_amdgcn_permlane16_swap(a,b,false,false); return (pr2){r[0],r[1]}; }
DI pr2 sw32(unsigned a,unsigned b){ auto r=__builtin_amdgcn_permlane32_swap(a,b,false,false); return (pr2){r[0],r[1]}; }
DI float mx2(float a,float b){ return __builtin_elementwise_maximum(a,b); }
DI float mx8(const f32x4& a,const f32x4& b){ return mx2(mx2(mx2(mx2(a[0],a[1]),a[2]),mx2(mx2(a[3],b[0]),b[1])),mx2(b[2],b[3])); }
DI float mn2(float a,float b){ return __builtin_elementwise_minimum(a,b); }
DI float mn8(const f32x4& a,const f32x4& b){ return mn2(mn2(mn2(mn2(a[0],a[1]),a[2]),mn2(mn2(a[3],b[0]),b[1])),mn2(b[2],b[3])); }
DI void xr_max2(float& a, float& b){
  pr2 r=sw32(__float_as_uint(a),__float_as_uint(b)); float t=mx2(__uint_as_float(r[0]),__uint_as_float(r[1]));
  r=sw16(__float_as_uint(t),__float_as_uint(t)); t=mx2(__uint_as_float(r[0]),__uint_as_float(r[1]));
  r=sw32(__float_as_uint(t),__float_as_uint(t)); a=__uint_as_float(r[0]); b=__uint_as_float(r[1]); }
DI unsigned flag_ld(const unsigned* p){ return __hip_atomic_load(p, __ATOMIC_RELAXED, __HIP_MEMORY_SCOPE_AGENT); }
DI void flag_st(unsigned* p, unsigned v){ __hip_atomic_store(p, v, __ATOMIC_RELAXED, __HIP_MEMORY_SCOPE_AGENT); }
DI void poll_flags(const unsigned* flags, int first, int n, unsigned epoch, int lane){
  const unsigned* p = flags + (size_t)(first + (lane<n?lane:0))*FST;
  for(unsigned spins=0; spins<(1u<<22); ++spins){
    unsigned v=flag_ld(p);
    if(__all(v==epoch)) break;
    __builtin_amdgcn_s_sleep(1);
  }
}
#define STAMP(i)
#define RSTAMP(i)

struct Params {
  const u16* x; const u16* wqkv; const u16* wphi; const u16* wo; const u16* w1; const u16* b1; const u16* w2; const u16* b2; const u16* gw;
  u16* out; char* ws;
};

#define QLA 2
#define MT0 0
#define EVALL 1
constexpr int EVN=EVALL?7:14;
#define QNT 0
constexpr int TW=QNT?0:1;
namespace qkv {
constexpr int XSLOT=256*128, WSLOT=64*384;
constexpr int XOFF=0, WOFF=2*XSLOT;
constexpr int WPHI_OFF=WOFF+3*WSLOT;
constexpr int LDS_BYTES=WPHI_OFF+4096;
struct Ctx { unsigned aX[2], aW[2], vX[4], vW[3], vT, v7, v16, dT; i32x4 srdX, srdW, srd1, srd2; unsigned mX, mW, soffX, soffW, soff1, soff2;
};
DI void ev_w1(Ctx& c){ touch<0>(c.dT,c.v7,c.srd1,c.soff1); saddi<EVN*1024*128>(c.soff1); }
template<int E> DI void ev_16(Ctx& c){ c.soff2=E*16384*128; touch<0>(c.dT,c.v16,c.srd2,c.soff2); }

template<int SET,int RX,int RW,int RKS,bool READ,bool DOX,int DXS,bool DOW,int DWS,bool TCH,int EV>
DI void step(Ctx& c, f32x4 (&acc)[3][2][4], u32x4 (&fX)[2][4], u32x4 (&fW)[2][3][2]){
  constexpr int NX=SET^1;
  u32x2 wl[3][2], wh[3][2];
  auto RXf=[&](auto mc){ constexpr int m=decltype(mc)::value; if constexpr(READ) ds_b128<RX*XSLOT+m*2048>(fX[NX][m], c.aX[RKS]); };
  auto RWf=[&](auto sc,auto ec,auto tc){ constexpr int s=decltype(sc)::value,e=decltype(ec)::value,t=decltype(tc)::value;
    if constexpr(READ){ constexpr int o=RW*WSLOT+RKS*12288+s*1024;
      if constexpr(t==0) ds_tr<o>(wl[s][e], c.aW[e]); else { ds_tr<o+512>(wh[s][e], c.aW[e]); fW[NX][s][e]=cat(wl[s][e],wh[s][e]); } } };
  auto MX=[&](){ if constexpr(DOX) set_m0<DXS*XSLOT>(c.mX); };
  auto MW=[&](){ if constexpr(DOW) set_m0<DWS*WSLOT>(c.mW); };
  auto DX=[&](auto kc){ constexpr int k=decltype(kc)::value; if constexpr(DOX) dma<k*1024>(c.vX[k], c.srdX, c.soffX); };
  auto DW=[&](auto kc){ constexpr int k=decltype(kc)::value; if constexpr(DOW) dma<k*1024,QNT>(c.vW[k], c.srdW, c.soffW); };
  auto MM=[&](auto sc,auto ec,auto mc){ constexpr int s=decltype(sc)::value,e=decltype(ec)::value,m=decltype(mc)::value;
    if constexpr(s<2) mfma(acc[s][e][m], fW[SET][s][e], fX[SET][m]); else mfma(acc[s][e][m], fX[SET][m], fW[SET][s][e]); };
  MM(IC(0),IC(0),IC(0)); RXf(IC(0)); MX();
  MM(IC(0),IC(0),IC(1)); RWf(IC(0),IC(0),IC(0)); DX(IC(0));
  MM(IC(0),IC(0),IC(2)); RWf(IC(0),IC(0),IC(1));
  MM(IC(0),IC(0),IC(3)); RXf(IC(1)); DX(IC(1));
  MM(IC(0),IC(1),IC(0)); RWf(IC(0),IC(1),IC(0));
  MM(IC(0),IC(1),IC(1)); RWf(IC(0),IC(1),IC(1)); DX(IC(2));
  MM(IC(0),IC(1),IC(2)); RXf(IC(2));
  MM(IC(0),IC(1),IC(3)); RXf(IC(3)); DX(IC(3));
  MM(IC(1),IC(0),IC(0)); RWf(IC(1),IC(0),IC(0)); MW();
  MM(IC(1),IC(0),IC(1)); RWf(IC(1),IC(0),IC(1)); DW(IC(0));
  MM(IC(1),IC(0),IC(2)); RWf(IC(1),IC(1),IC(0));
  MM(IC(1),IC(0),IC(3)); RWf(IC(1),IC(1),IC(1)); DW(IC(1));
  MM(IC(1),IC(1),IC(0)); RWf(IC(2),IC(0),IC(0));
  MM(IC(1),IC(1),IC(1)); RWf(IC(2),IC(0),IC(1)); DW(IC(2));
  MM(IC(1),IC(1),IC(2)); RWf(IC(2),IC(1),IC(0));
  MM(IC(1),IC(1),IC(3)); RWf(IC(2),IC(1),IC(1));
  if constexpr(TCH) touch<0>(c.dT, c.vT, c.srdW, c.soffW);
  MM(IC(2),IC(0),IC(0));
  if constexpr(EV==1) ev_w1(c);
  if constexpr(EV==6){ ev_w1(c); ev_16<0>(c); }
  else if constexpr(EV>=2) ev_16<EV-2>(c);
  if constexpr(DOX) saddi<128>(c.soffX);
  if constexpr(DOW) saddi<64*6144>(c.soffW);
  MM(IC(2),IC(0),IC(1)); MM(IC(2),IC(0),IC(2)); MM(IC(2),IC(0),IC(3));
  MM(IC(2),IC(1),IC(0)); MM(IC(2),IC(1),IC(1)); MM(IC(2),IC(1),IC(2)); MM(IC(2),IC(1),IC(3));
  if constexpr(READ) waitl<0>();
}
template<int T6,int VM,bool DOX,bool DOW,int EV>
DI void iter(Ctx& c, f32x4 (&acc)[3][2][4], u32x4 (&fX)[2][4], u32x4 (&fW)[2][3][2]){
  step<0,T6&1,T6%3,1,true,false,0,false,0,false,0>(c,acc,fX,fW);
  if constexpr(VM>=0){ waitv<VM>(); bar(); }
  step<1,(T6+1)&1,(T6+1)%3,0,(VM>=0),DOX,T6&1,DOW,T6%3,TW&&DOW&&((T6&1)==0),EV>(c,acc,fX,fW);
}

template<int N,int M0>
DI void feat(const u32x4 (&Ap)[2], const f32x4 (&a)[2][4], f32x2 (&ep)[N][4], f32x2 (&en)[N][4]){
  constexpr float L2E=1.4426950408889634f;
  const f32x4 zero={0,0,0,0};
  f32x4 P0[N], P1[N]; float mx[N], nm[N];
  #pragma unroll
  for(int i=0;i<N;i++){ const int m=M0+i;
    u32x4 B; B[0]=cvtpk(lo2(a[0][m])); B[1]=cvtpk(hi2(a[0][m])); B[2]=cvtpk(lo2(a[1][m])); B[3]=cvtpk(hi2(a[1][m]));
    P0[i]=mfma_b(Ap[0],B,zero); P1[i]=mfma_b(Ap[1],B,zero); }
  #pragma unroll
  for(int i=0;i<N;i++){ mx[i]=mx8(P0[i],P1[i]); nm[i]=-mn8(P0[i],P1[i]); }
  pr2 r[N]; float t[N];
  #pragma unroll
  for(int i=0;i<N;i++){ r[i]=sw32(__float_as_uint(mx[i]),__float_as_uint(nm[i])); t[i]=mx2(__uint_as_float(r[i][0]),__uint_as_float(r[i][1])); }
  #pragma unroll
  for(int i=0;i<N;i++){ r[i]=sw16(__float_as_uint(t[i]),__float_as_uint(t[i])); t[i]=mx2(__uint_as_float(r[i][0]),__uint_as_float(r[i][1])); }
  #pragma unroll
  for(int i=0;i<N;i++){ r[i]=sw32(__float_as_uint(t[i]),__float_as_uint(t[i])); }
  #pragma unroll
  for(int i=0;i<N;i++){
    const f32x2 cp=bc2(__uint_as_float(r[i][0])*-L2E), cn=bc2(__uint_as_float(r[i][1])*-L2E), kp=bc2(L2E), kn=bc2(-L2E);
    const f32x2 q[4]={lo2(P0[i]),hi2(P0[i]),lo2(P1[i]),hi2(P1[i])};
    #pragma unroll
    for(int k=0;k<4;k++){
      const f32x2 tp=fma2(q[k],kp,cp), tn=fma2(q[k],kn,cn);
      ep[i][k]=(f32x2){__builtin_amdgcn_exp2f(tp[0]),__builtin_amdgcn_exp2f(tp[1])};
      en[i][k]=(f32x2){__builtin_amdgcn_exp2f(tn[0]),__builtin_amdgcn_exp2f(tn[1])};
    } }
}
DI u32x4 pack8(const f32x2 (&v)[4]){ return (u32x4){cvtpk(v[0]),cvtpk(v[1]),cvtpk(v[2]),cvtpk(v[3])}; }
DI float dotk(unsigned x,unsigned k,float c){ return __builtin_amdgcn_fdot2_f32_bf16(__builtin_bit_cast(bf16x2_t,x),__builtin_bit_cast(bf16x2_t,k),c,false); }

DI void epilogue(const Params& p, char* smem, f32x4 (&acc)[3][2][4], int rt, int ct, int w, unsigned& dT, unsigned& epoch_out, unsigned long long* stamps){
  const unsigned lds0=(unsigned)(size_t)(LAS char*)smem;
  LAS char* sm=(LAS char*)smem;
  const int tid=otid(), l=tid&63;
  const int g=l>>4,q=(l&15)>>2,pp=l&3,i16=l&15;
  const int wr=w&3, wh=w>>2;
  constexpr int TPHI=0, VREG=65536, SHR=98304;
  char* ws=p.ws;
  unsigned* flags=(unsigned*)(ws+WS_FLG);
  unsigned gen; gld32_sc1(gen, flags);
  u32x4 Ap[2];
  { const unsigned b=lds0+WPHI_OFF+wh*2048+(4*g+q)*64+pp*8;
    u32x2 l0,h0,l1,h1; ds_tr<0>(l0,b); ds_tr<1024>(h0,b); ds_tr<32>(l1,b); ds_tr<32+1024>(h1,b);
    #pragma unroll
    for(int mp=0;mp<2;mp++)
    #pragma unroll
    for(int e=0;e<2;e++){
      u32x4 fv; fv[0]=cvtpk(lo2(acc[2][e][2*mp])); fv[1]=cvtpk(hi2(acc[2][e][2*mp])); fv[2]=cvtpk(lo2(acc[2][e][2*mp+1])); fv[3]=cvtpk(hi2(acc[2][e][2*mp+1]));
      *(LAS u32x4*)(sm+VREG+w*4096+(mp*2+e)*1024+l*16)=fv;
    }
    asm volatile("s_waitcnt lgkmcnt(0)" : "+v"(l0),"+v"(h0),"+v"(l1),"+v"(h1) :: "memory");
    Ap[0]=cat(l0,h0); Ap[1]=cat(l1,h1); }
  const u32x4 ones={0x3F803F80u,0x3F803F80u,0x3F803F80u,0x3F803F80u};
  const f32x4 zero={0,0,0,0};
  STAMP(9);
  { const int swp=(((i16>>1)&3)<<2)|(((i16>>3)&1)<<1)|(i16&1);
    f32x2 ep[4][4], en[4][4];
    feat<4,0>(Ap,acc[1],ep,en);
    f32x4 sp[4], sn[4];
    #pragma unroll
    for(int m=0;m<4;m++){ sp[m]=mfma_b(ones,pack8(ep[m]),zero); sn[m]=mfma_b(ones,pack8(en[m]),zero); }
    #pragma unroll
    for(int m=0;m<4;m++){
      const f32x2 rp=bc2(__builtin_amdgcn_rcpf(sp[m][0])), rn=bc2(__builtin_amdgcn_rcpf(sn[m][0]));
      LAS char* row=sm+TPHI+w*8192+(16*m+i16)*128;
      *(LAS u32x2*)(row+(((0+g)^swp)*8))=(u32x2){cvtpk(ep[m][0]*rp),cvtpk(ep[m][1]*rp)};
      *(LAS u32x2*)(row+(((4+g)^swp)*8))=(u32x2){cvtpk(ep[m][2]*rp),cvtpk(ep[m][3]*rp)};
      *(LAS u32x2*)(row+(((8+g)^swp)*8))=(u32x2){cvtpk(en[m][0]*rn),cvtpk(en[m][1]*rn)};
      *(LAS u32x2*)(row+(((12+g)^swp)*8))=(u32x2){cvtpk(en[m][2]*rn),cvtpk(en[m][3]*rn)};
    } }
  STAMP(10);
  __syncthreads();
  STAMP(4);
  f32x4 kv0={0,0,0,0},kv1={0,0,0,0},zz={0,0,0,0};
  { const int r15=4*g+q; const int swr=(((r15>>1)&3)<<2)|(((r15>>3)&1)<<1)|(r15&1);
    const unsigned ab=lds0+TPHI+wh*4*8192+r15*128+(((4*wr+pp)^swr)*8);
    const unsigned vb=lds0+VREG+wh*4*4096+l*16;
    u32x2 a[4][4]; u32x4 v[4][4];
    auto rd=[&](auto sc){ constexpr int s_=decltype(sc)::value;
      ds_tr<s_*8192+0>(a[s_][0],ab); ds_tr<s_*8192+2048>(a[s_][1],ab); ds_tr<s_*8192+4096>(a[s_][2],ab); ds_tr<s_*8192+6144>(a[s_][3],ab);
      ds_b128<s_*4096>(v[s_][0],vb); ds_b128<s_*4096+1024>(v[s_][1],vb); ds_b128<s_*4096+2048>(v[s_][2],vb); ds_b128<s_*4096+3072>(v[s_][3],vb); };
    rd(IC(0)); rd(IC(1)); rd(IC(2)); rd(IC(3));
    waitl<0>();
    #pragma unroll
    for(int s_=0;s_<4;s_++){
      #pragma unroll
      for(int k=0;k<4;k++){ pin(a[s_][k]); pin(v[s_][k]); }
      const u32x4 A0=cat(a[s_][0],a[s_][1]), A1=cat(a[s_][2],a[s_][3]);
      kv0=mfma_b(A0,v[s_][0],kv0); kv1=mfma_b(A0,v[s_][1],kv1); zz=mfma_b(A0,ones,zz);
      kv0=mfma_b(A1,v[s_][2],kv0); kv1=mfma_b(A1,v[s_][3],kv1); zz=mfma_b(A1,ones,zz); }
  }
  const __amdgpu_buffer_rsrc_t rKV=make_rsrc(ws+WS_KVP,16*8*8*1024), rZ=make_rsrc(ws+WS_ZP,16*8*2*64*4);
  st_sc1(rKV,(unsigned)(((ct*8+rt)*8+w)*1024+l*16),(u32x4){cvtpk(lo2(kv0)),cvtpk(hi2(kv0)),cvtpk(lo2(kv1)),cvtpk(hi2(kv1))});
  if(i16==0) st_sc1(rZ,(unsigned)((((ct*8+rt)*2+wh)*64+16*wr+4*g)*4),__builtin_bit_cast(u32x4,zz));
  u32x4 phq[4][2]; float rq[4][2];
  auto qblk=[&](auto nc,auto mc){ constexpr int n=decltype(nc)::value, m0=decltype(mc)::value;
    f32x2 ep[n][4], en[n][4];
    feat<n,m0>(Ap,acc[0],ep,en);
    #pragma unroll
    for(int i=0;i<n;i++){ phq[m0+i][0]=pack8(ep[i]); phq[m0+i][1]=pack8(en[i]); }
    #pragma unroll
    for(int i=0;i<n;i++){ const f32x4 sp=mfma_b(ones,phq[m0+i][0],zero), sn=mfma_b(ones,phq[m0+i][1],zero);
      rq[m0+i][0]=__builtin_amdgcn_rcpf(sp[0]); rq[m0+i][1]=__builtin_amdgcn_rcpf(sn[0]); } };
  qblk(IC(2),IC(0));
  asm volatile("s_waitcnt vmcnt(0)" : "+v"(gen), "+v"(dT) :: "memory");
  const unsigned epoch=rfl(gen)+1u; epoch_out=epoch;
  __syncthreads();
  if(tid==0) flag_st(flags+(FLG_KV+ct*8+rt)*FST, epoch);
  STAMP(5);
  qblk(IC(1),IC(2));
  poll_flags(flags, FLG_KV+ct*8, 8, epoch, l);
  STAMP(6);
  { u32x4 t0[8],tz[8];
    #pragma unroll
    for(int r=0;r<8;r++){ t0[r]=ld_sc1(rKV,(unsigned)(((ct*8+r)*8+w)*1024+l*16)); tz[r]=ld_sc1(rZ,(unsigned)((((ct*8+r)*2+wh)*64+16*wr+4*g)*4)); }
    asm volatile("" ::: "memory");
    qblk(IC(1),IC(3));
    float s[8]={0,0,0,0,0,0,0,0}; f32x4 sz={0,0,0,0};
    unsigned klo=0x00003F80u, khi=0x3F800000u; asm volatile("" : "+v"(klo), "+v"(khi));
    #pragma unroll
    for(int r=0;r<8;r++){
      #pragma unroll
      for(int k=0;k<4;k++){ s[2*k]=dotk(t0[r][k],klo,s[2*k]); s[2*k+1]=dotk(t0[r][k],khi,s[2*k+1]); }
      sz+=__builtin_bit_cast(f32x4,tz[r]); }
    LAS char* sb=sm+SHR+w*1536+l*8;
    *(LAS u32x2*)(sb)=(u32x2){cvtpk(s[0],s[1]),cvtpk(s[2],s[3])};
    *(LAS u32x2*)(sb+512)=(u32x2){cvtpk(s[4],s[5]),cvtpk(s[6],s[7])};
    *(LAS u32x2*)(sb+1024)=(u32x2){cvtpk(lo2(sz)),cvtpk(hi2(sz))};
  }
  __syncthreads();
  STAMP(7);
  { u32x4 kf[2][2], zf[2];
    #pragma unroll
    for(int sg=0;sg<2;sg++){
      LAS char* b0=sm+SHR+(wh*4+2*sg)*1536+l*8; LAS char* b1=b0+1536;
      kf[sg][0]=cat(*(LAS u32x2*)(b0),*(LAS u32x2*)(b1)); kf[sg][1]=cat(*(LAS u32x2*)(b0+512),*(LAS u32x2*)(b1+512)); zf[sg]=cat(*(LAS u32x2*)(b0+1024),*(LAS u32x2*)(b1+1024)); }
    const __amdgpu_buffer_rsrc_t rO=make_rsrc(ws+WS_O,S*D*2);
    const int offg=(g&1)*32+(g>>1)*16;
    f32x4 n0p[4],n0n[4],n1p[4],n1n[4],dp[4],dn[4];
    #pragma unroll
    for(int m=0;m<4;m++){
      dp[m]=mfma_b(zf[0],phq[m][0],zero); dn[m]=mfma_b(zf[1],phq[m][1],zero);
      n0p[m]=mfma_b(kf[0][0],phq[m][0],zero); n0n[m]=mfma_b(kf[1][0],phq[m][1],zero);
      n1p[m]=mfma_b(kf[0][1],phq[m][0],zero); n1n[m]=mfma_b(kf[1][1],phq[m][1],zero); }
    #pragma unroll
    for(int m=0;m<4;m++){
      const float rd=__builtin_amdgcn_rcpf(dp[m][0]*rq[m][0]+dn[m][0]*rq[m][1]+1e-6f);
      const f32x2 a=bc2(rq[m][0]*rd), b=bc2(rq[m][1]*rd);
      const unsigned a0=cvtpk(fma2(lo2(n0p[m]),a,lo2(n0n[m])*b)), a1=cvtpk(fma2(hi2(n0p[m]),a,hi2(n0n[m])*b));
      const unsigned b0=cvtpk(fma2(lo2(n1p[m]),a,lo2(n1n[m])*b)), b1=cvtpk(fma2(hi2(n1p[m]),a,hi2(n1n[m])*b));
      const pr2 r0=sw16(a0,b0), r1=sw16(a1,b1);
      const u32x4 ov={r0[0],r1[0],r0[1],r1[1]};
      const int tok=rt*256+64*wr+16*m+i16;
      st_sc1(rO,(unsigned)((ct*2+wh)*131072+tok*64+offg),ov);
    }
  }
  STAMP(8);
}

DI unsigned run(const Params& p, char* smem, int rt, int ct, unsigned long long* stamps){
  const unsigned lds0=(unsigned)(size_t)(LAS char*)smem;
  const int tid=threadIdx.x, w=rfl(tid>>6), l=tid&63;
  const int g=l>>4,q=(l&15)>>2,pp=l&3,i16=l&15;
  const int wr=w&3, wh=w>>2;
  Ctx c; f32x4 acc[3][2][4]; u32x4 fX[2][4], fW[2][3][2];
  c.srdX=make_srd(p.x,S*D*2); c.srdW=make_srd(p.wqkv,D*3*D*2);
  c.mX=rfl(lds0+XOFF+w*4096); c.mW=rfl(lds0+WOFF+w*3072);
  #pragma unroll
  for(int k=0;k<4;k++){ const int row=32*w+8*k+(l>>3); const int ch=(l&7)^((row>>1)&7); c.vX[k]=(rt*256+row)*2048+ch*16-1024*k; }
  #pragma unroll
  for(int s=0;s<3;s++){ const int row=8*w+(l>>3); const int sw=((row>>1)&1)|(((row>>3)&1)<<1); const int pos=l&7; const int ch=(((pos>>1)^sw)<<1)|(pos&1);
    c.vW[s]=row*6144+s*2048+ct*128+ch*16-1024*s; }
  { const int idx=w*6+l, tl=idx/24, rem=idx-tl*24, row=8*rt+rem/3, s=rem-(rem/3)*3;
    c.vT=(l<6)? (unsigned)(((tl+QLA)*64+row)*6144+s*2048+ct*128) : 0x80000000u; c.dT=0; }
  { const int gwv=(ct*8+rt)*8+w;
    c.v7=(l<EVN)? (unsigned)((gwv*EVN+l)*128) : 0x80000000u; c.v16=(l<16)? (unsigned)((gwv*16+l)*128) : 0x80000000u;
    c.srd1=make_srd(p.w1,S*F*2); c.soff1=(4+MT0)*64*7168; c.srd2=make_srd(p.w2,F*S*2); c.soff2=0; }
  #pragma unroll
  for(int ks=0;ks<2;ks++) c.aX[ks]=lds0+XOFF+(64*wr+i16)*128+(((4*ks+g)^((i16>>1)&7))*16);
  { const int sw2=((q>>1)&1)|((g&1)<<1);
    #pragma unroll
    for(int e=0;e<2;e++) c.aW[e]=lds0+WOFF+g*3072+q*128+(((2*wh+e)^sw2)*32)+8*pp; }
  #pragma unroll
  for(int s=0;s<3;s++)
  #pragma unroll
  for(int e=0;e<2;e++)
  #pragma unroll
  for(int m=0;m<4;m++){ acc[s][e][m]=(f32x4){0,0,0,0}; pin(acc[s][e][m]); }
  STAMP(1);
  c.soffX=0; c.soffW=0;
  if(w<4){ i32x4 srdP=make_srd(p.wphi,32*32*32*2); set_m0<0>(rfl(lds0+WPHI_OFF+w*1024)); asm volatile("s_nop 4"); dma<0>((unsigned)(ct*4096+w*1024+l*16), srdP, 0u); }
  auto tileX=[&](auto tc){ constexpr int T=decltype(tc)::value;
    set_m0<T*XSLOT>(c.mX); nop1(); dma<0>(c.vX[0],c.srdX,c.soffX); dma<1024>(c.vX[1],c.srdX,c.soffX); dma<2048>(c.vX[2],c.srdX,c.soffX); dma<3072>(c.vX[3],c.srdX,c.soffX);
    saddi<128>(c.soffX); };
  auto tileW=[&](auto tc){ constexpr int T=decltype(tc)::value;
    set_m0<T*WSLOT>(c.mW); nop1(); dma<0,QNT>(c.vW[0],c.srdW,c.soffW); dma<1024,QNT>(c.vW[1],c.srdW,c.soffW); dma<2048,QNT>(c.vW[2],c.srdW,c.soffW);
    saddi<64*6144>(c.soffW); };
  tileW(IC(0)); tileX(IC(0));
  if constexpr(TW) touch<0>(c.dT,c.vT,c.srdW,c.soffW);
  tileX(IC(1)); tileW(IC(1));
  waitv<7+TW>(); bar();
  {
    ds_b128<0>(fX[0][0],c.aX[0]); ds_b128<2048>(fX[0][1],c.aX[0]); ds_b128<4096>(fX[0][2],c.aX[0]); ds_b128<6144>(fX[0][3],c.aX[0]);
    auto rd=[&](auto sc,auto ec){ constexpr int s=decltype(sc)::value,e=decltype(ec)::value; u32x2 lo,hi; ds_tr<s*1024>(lo,c.aW[e]); ds_tr<s*1024+512>(hi,c.aW[e]); fW[0][s][e]=cat(lo,hi); };
    rd(IC(0),IC(0)); rd(IC(0),IC(1)); rd(IC(1),IC(0)); rd(IC(1),IC(1)); rd(IC(2),IC(0)); rd(IC(2),IC(1));
    waitl<0>(); }
  tileW(IC(2)); ev_w1(c);
  STAMP(2); RSTAMP(26);
  for(int t=0;t<12;t+=6){
    iter<0,4,true,true,1>(c,acc,fX,fW); iter<1,4+TW,true,true,1>(c,acc,fX,fW); iter<2,4,true,true,1>(c,acc,fX,fW);
    iter<3,4+TW,true,true,1>(c,acc,fX,fW); iter<4,4,true,true,1>(c,acc,fX,fW); iter<5,4+TW,true,true,1>(c,acc,fX,fW); }
  iter<0,4,true,true,EVALL?6:2>(c,acc,fX,fW);
  iter<1,4+EVALL+TW,true,false,2+2>(c,acc,fX,fW);
  iter<2,1,false,false,2+1>(c,acc,fX,fW);
  iter<3,-1,false,false,2+3>(c,acc,fX,fW);
  ev_16<4>(c);
  c.srd2=make_srd(p.wo,D*D*2); ev_16<0>(c);
  asm volatile("s_nop 7\n\ts_nop 7" ::: "memory");
  #pragma unroll
  for(int s=0;s<3;s++)
  #pragma unroll
  for(int e=0;e<2;e++)
  #pragma unroll
  for(int m=0;m<4;m++) pin(acc[s][e][m]);
  STAMP(3); RSTAMP(27);
  bar();
  unsigned epoch;
  epilogue(p,smem,acc,rt,ct,w,c.dT,epoch,stamps);
  return epoch;
}
}

#define MLA 3
#define MNT0 0
#define HPOL 2
namespace mix1 {
constexpr int HSLOT=64*256, WSLOT=64*448;
constexpr int HOFF=0, WOFF=3*HSLOT;
constexpr int LDS_BYTES=WOFF+4*WSLOT;
struct Ctx { unsigned bH[4], bW[2][2], bW6[2], vH[2], vW[4]; i32x4 srdH, srdW; unsigned mH, mW, soffH, soffW; };
template<int POL> DI void dma_w3(Ctx& c){
  if constexpr(POL==1) asm volatile("s_mov_b32 exec_hi, 0\n\tbuffer_load_dwordx4 %0, %1, %2 offen offset:3072 nt lds\n\ts_mov_b32 exec_hi, -1" :: "v"(c.vW[3]), "s"(c.srdW), "s"(c.soffW) : "memory");
  else asm volatile("s_mov_b32 exec_hi, 0\n\tbuffer_load_dwordx4 %0, %1, %2 offen offset:3072 lds\n\ts_mov_b32 exec_hi, -1" :: "v"(c.vW[3]), "s"(c.srdW), "s"(c.soffW) : "memory"); }
template<int SET,int RH,int RW,bool READ,bool DOH,int DH,bool DOW,int DW,int VM>
DI void iter(Ctx& c, f32x4 (&acc)[7][4], u32x4 (&fH)[2][4], u32x4 (&fW)[2][7]){
  constexpr int NX=SET^1;
  if constexpr(VM>=0){ waitv<VM>(); bar(); }
  u32x2 hl[4],hh[4],wl[7],wh[7];
  auto RHf=[&](auto ic,auto hc){ constexpr int i=decltype(ic)::value,h=decltype(hc)::value;
    if constexpr(READ){ if constexpr(h==0) ds_tr<RH*HSLOT>(hl[i],c.bH[i]); else { ds_tr<RH*HSLOT+1024>(hh[i],c.bH[i]); fH[NX][i]=cat(hl[i],hh[i]); } } };
  auto RWf=[&](auto ic,auto hc){ constexpr int i=decltype(ic)::value,h=decltype(hc)::value;
    constexpr int off=(RW&1)*WSLOT+((i<6)?(i&~1)*32:0);
    if constexpr(READ){ const unsigned a=(i<6)? c.bW[RW>>1][i&1] : c.bW6[RW>>1];
      if constexpr(h==0) ds_tr<off>(wl[i],a); else { ds_tr<off+4*448>(wh[i],a); fW[NX][i]=cat(wl[i],wh[i]); } } };
  auto MH=[&](){ if constexpr(DOH) set_m0<DH*HSLOT>(c.mH); };
  auto MW=[&](){ if constexpr(DOW) set_m0<DW*WSLOT>(c.mW); };
  auto DHf=[&](auto kc){ constexpr int k=decltype(kc)::value; if constexpr(DOH) dma<k*1024,HPOL>(c.vH[k],c.srdH,c.soffH); };
  auto DWf=[&](auto kc){ constexpr int k=decltype(kc)::value; if constexpr(DOW){ if constexpr(k<3) dma<k*1024>(c.vW[k],c.srdW,c.soffW); else dma_w3<0>(c); } };
  auto MM=[&](auto ac,auto bc){ constexpr int a=decltype(ac)::value,b=decltype(bc)::value; mfma(acc[a][b],fW[SET][a],fH[SET][b]); };
  MM(IC(0),IC(0)); RHf(IC(0),IC(0)); MH();
  MM(IC(0),IC(1)); RHf(IC(0),IC(1));
  MM(IC(0),IC(2)); RWf(IC(0),IC(0)); DHf(IC(0));
  MM(IC(0),IC(3)); RWf(IC(0),IC(1));
  MM(IC(1),IC(0)); RHf(IC(1),IC(0));
  MM(IC(1),IC(1)); RHf(IC(1),IC(1)); DHf(IC(1));
  MM(IC(1),IC(2)); RWf(IC(1),IC(0));
  MM(IC(1),IC(3)); RWf(IC(1),IC(1)); MW();
  MM(IC(2),IC(0)); RHf(IC(2),IC(0));
  MM(IC(2),IC(1)); RHf(IC(2),IC(1)); DWf(IC(0));
  MM(IC(2),IC(2)); RWf(IC(2),IC(0));
  MM(IC(2),IC(3)); RWf(IC(2),IC(1));
  MM(IC(3),IC(0)); RHf(IC(3),IC(0));
  MM(IC(3),IC(1)); RHf(IC(3),IC(1)); DWf(IC(1));
  MM(IC(3),IC(2)); RWf(IC(3),IC(0));
  MM(IC(3),IC(3)); RWf(IC(3),IC(1));
  MM(IC(4),IC(0)); RWf(IC(4),IC(0));
  MM(IC(4),IC(1)); RWf(IC(4),IC(1)); DWf(IC(2));
  MM(IC(4),IC(2)); RWf(IC(5),IC(0));
  MM(IC(4),IC(3)); RWf(IC(5),IC(1));
  MM(IC(5),IC(0)); RWf(IC(6),IC(0));
  MM(IC(5),IC(1)); RWf(IC(6),IC(1)); DWf(IC(3));
  if constexpr(DOH) saddi<64*2048>(c.soffH);
  if constexpr(DOW) saddi<64*7168>(c.soffW);
  MM(IC(5),IC(2)); MM(IC(5),IC(3));
  MM(IC(6),IC(0)); MM(IC(6),IC(1)); MM(IC(6),IC(2)); MM(IC(6),IC(3));
  if constexpr(READ) waitl<0>();
}
template<int T,bool DOH,bool DOW,int VM,bool READ=true>
DI void it12(Ctx& c, f32x4 (&acc)[7][4], u32x4 (&fH)[2][4], u32x4 (&fW)[2][7]){
  iter<T&1,(T+1)%3,(T+1)&3,READ,DOH,T%3,DOW,T&3,VM>(c,acc,fH,fW); }

DI f32x2 gelu2(f32x2 x){
  const float k1=-2.0f*0.7978845608028654f*1.4426950408889634f, k2=k1*0.044715f;
  const f32x2 t=x*fma2(x*x,bc2(k2),bc2(k1));
  const f32x2 e={__builtin_amdgcn_exp2f(t[0]),__builtin_amdgcn_exp2f(t[1])};
  const f32x2 d=e+bc2(1.0f);
  return x*(f32x2){__builtin_amdgcn_rcpf(d[0]),__builtin_amdgcn_rcpf(d[1])};
}
template<int KG>
DI void epilogue(const Params& p, LAS char* sm, f32x4 (&acc)[7][4], int w, int dt, int ft){
  const int tid=otid(), l=tid&63, dh=w&1, fh=(w>>1)&1;
  const int g=l>>4, i16=l&15;
  { LAS char* wb=sm+w*16384+l*16;
    #pragma unroll
    for(int a=0;a<6;a++){ *(LAS f32x4*)(wb+(a*2+0)*1024)=acc[a][2*(1-KG)]; *(LAS f32x4*)(wb+(a*2+1)*1024)=acc[a][2*(1-KG)+1]; }
    #pragma unroll
    for(int b=0;b<4;b++) *(LAS f32x4*)(wb+(12+b)*1024)=acc[6][b]; }
  __syncthreads();
  const __amdgpu_buffer_rsrc_t rU=make_rsrc(p.ws+WS_U,D*F*2);
  LAS char* rb=sm+(w^4)*16384+l*16;
  const unsigned ub=(unsigned)((dt*112+ft*7)*8192+(64*dh+32*KG+i16)*64+(g&1)*32+(g>>1)*16);
  auto put=[&](const f32x4& x0,const f32x4& x1,unsigned off){
    const pr2 r0=sw16(cvtpk(gelu2(lo2(x0))),cvtpk(gelu2(lo2(x1)))), r1=sw16(cvtpk(gelu2(hi2(x0))),cvtpk(gelu2(hi2(x1))));
    st_sc1(rU, ub+off, (u32x4){r0[0],r1[0],r0[1],r1[1]}); };
  { const int bo=(12+2*KG+fh)*1024+l*16;
    const f32x4 t6=*(LAS f32x4*)(sm+(w&~6)*16384+bo)+*(LAS f32x4*)(sm+((w&~6)|4)*16384+bo);
    const f32x4 t7=*(LAS f32x4*)(sm+((w&~6)|2)*16384+bo)+*(LAS f32x4*)(sm+((w&~6)|6)*16384+bo);
    put(t6,t7,(unsigned)(3*8192+fh*1024)); }
  #pragma unroll
  for(int pr=0;pr<3;pr++)
  #pragma unroll
  for(int b=0;b<2;b++)
    put(acc[2*pr][2*KG+b]+*(LAS f32x4*)(rb+(4*pr+b)*1024), acc[2*pr+1][2*KG+b]+*(LAS f32x4*)(rb+(4*pr+2+b)*1024), (unsigned)((fh*4+pr)*8192+b*1024));
}

DI float dpp_sum(float x){
  auto add=[&](auto cc,auto rm){ x+=__int_as_float(__builtin_amdgcn_update_dpp(0,__float_as_int(x),decltype(cc)::value,decltype(rm)::value,0xf,true)); };
  add(IC(0x111),IC(0xf)); add(IC(0x112),IC(0xf)); add(IC(0x114),IC(0xf)); add(IC(0x118),IC(0xf));
  add(IC(0x142),IC(0xa)); add(IC(0x143),IC(0xc));
  return __int_as_float(__builtin_amdgcn_readlane(__float_as_int(x),63)); }
DI float dot2(unsigned a,unsigned b,float c){ return __builtin_amdgcn_fdot2_f32_bf16(__builtin_bit_cast(bf16x2_t,a),__builtin_bit_cast(bf16x2_t,b),c,false); }

DI unsigned run(const Params& p, char* smem, int dt, int ft, int mi, unsigned long long* stamps){
  const unsigned lds0=(unsigned)(size_t)(LAS char*)smem;
  const int tid=threadIdx.x, w=rfl(tid>>6), l=tid&63;
  const int g=l>>4,q=(l&15)>>2,pp=l&3;
  const int dh=w&1, fh=(w>>1)&1, kg=w>>2;
  const int d0=dt*128, f0=ft*224;
  unsigned* flags=(unsigned*)(p.ws+WS_FLG);
  STAMP(9);
  unsigned gen; u32x4 gw0,gw1,xa[2][2]; u32x2 bb[7];
  gld32_sc1(gen, flags);
  { const u16* xr=p.x+(size_t)(mi*16+2*w)*D+l*8;
    gld128<0>(xa[0][0],xr); gld128<1024>(xa[0][1],xr); gld128<2048>(xa[1][0],xr); gld128<3072>(xa[1][1],xr);
    gld128<0>(gw0,p.gw+l*8); gld128<1024>(gw1,p.gw+l*8);
    const u16* bp=p.b1+f0+4*g+fh*128;
    gld64<0>(bb[0],bp); gld64<32>(bb[1],bp); gld64<64>(bb[2],bp); gld64<96>(bb[3],bp); gld64<128>(bb[4],bp); gld64<160>(bb[5],bp);
    gld64<0>(bb[6],p.b1+f0+4*g+(6+fh)*16); }
  Ctx c; f32x4 acc[7][4]; u32x4 fH[2][4], fW[2][7];
  c.srdH=make_srd(p.ws+WS_H,S*D*2); c.srdW=make_srd(p.w1,S*F*2);
  c.mH=rfl(lds0+HOFF+w*2048); c.mW=rfl(lds0+WOFF+w*3584);
  #pragma unroll
  for(int k=0;k<2;k++){ const int row=8*w+4*k+(l>>4); const int sw=(row&3)|(((row>>3)&1)<<2); const int pos=l&15; const int ch=(((pos>>1)^sw)<<1)|(pos&1);
    c.vH[k]=row*2048+d0*2+ch*16-1024*k; }
  #pragma unroll
  for(int k=0;k<4;k++){ const int n=224*w+64*k+l; const int row=n/28,pos=n-row*28; const int ch=pos^(((row>>3)&1)<<1); c.vW[k]=(unsigned)(row*7168+f0*2+ch*16-1024*k); }
  c.soffH=0; c.soffW=0;
  { const int row=32*kg+8*g+q; const int sw=q|((g&1)<<2);
    #pragma unroll
    for(int e=0;e<4;e++) c.bH[e]=lds0+HOFF+row*256+(((4*dh+e)^sw)*32)+8*pp;
    const int b3=g&1;
    #pragma unroll
    for(int hi=0;hi<2;hi++){
      #pragma unroll
      for(int e=0;e<2;e++) c.bW[hi][e]=lds0+WOFF+hi*2*WSLOT+row*448+(((fh*8+e)^b3)*32)+8*pp;
      c.bW6[hi]=lds0+WOFF+hi*2*WSLOT+row*448+(((6+fh)^b3)*32)+8*pp; } }
  #pragma unroll
  for(int k=0;k<4;k++){ pin(c.vW[k]); pin(c.bH[k]); }
  pin(c.vH[0]); pin(c.vH[1]); pin(c.bW[0][0]); pin(c.bW[0][1]); pin(c.bW[1][0]); pin(c.bW[1][1]); pin(c.bW6[0]); pin(c.bW6[1]);
  auto tileW=[&](auto tc){ constexpr int T=decltype(tc)::value;
    set_m0<T*WSLOT>(c.mW); nop1(); dma<0,MNT0>(c.vW[0],c.srdW,c.soffW); dma<1024,MNT0>(c.vW[1],c.srdW,c.soffW); dma<2048,MNT0>(c.vW[2],c.srdW,c.soffW); dma_w3<MNT0>(c);
    saddi<64*7168>(c.soffW); };
  auto tileH=[&](auto tc){ constexpr int T=decltype(tc)::value;
    set_m0<T*HSLOT>(c.mH); nop1(); dma<0,2>(c.vH[0],c.srdH,c.soffH); dma<1024,2>(c.vH[1],c.srdH,c.soffH);
    saddi<64*2048>(c.soffH); };
  unsigned dT=0;
  waitv<7>();
  pin(gen); pin(gw0); pin(gw1); pin(xa[0][0]); pin(xa[0][1]); pin(xa[1][0]); pin(xa[1][1]);
  STAMP(10); RSTAMP(28);
  { const __amdgpu_buffer_rsrc_t rH=make_rsrc(p.ws+WS_H,S*D*2), rG=make_rsrc(p.ws+WS_G,S*4);
    float sum[2], z[2];
    #pragma unroll
    for(int r=0;r<2;r++){ float s=0.f, zz=0.f;
      #pragma unroll
      for(int i=0;i<4;i++){ s=dot2(xa[r][0][i],0x3F803F80u,s); zz=dot2(xa[r][0][i],gw0[i],zz); s=dot2(xa[r][1][i],0x3F803F80u,s); zz=dot2(xa[r][1][i],gw1[i],zz); }
      sum[r]=s; z[r]=zz; }
    #pragma unroll
    for(int r=0;r<2;r++){ sum[r]=dpp_sum(sum[r]); z[r]=dpp_sum(z[r]); }
    f32x2 v[2][8]; float qq[2];
    #pragma unroll
    for(int r=0;r<2;r++){ const f32x2 mean=bc2(sum[r]*(1.0f/D)); f32x2 q2={0.f,0.f};
      #pragma unroll
      for(int i=0;i<4;i++){ v[r][i]=bf2(xa[r][0][i])-mean; v[r][4+i]=bf2(xa[r][1][i])-mean; q2=fma2(v[r][i],v[r][i],q2); q2=fma2(v[r][4+i],v[r][4+i],q2); }
      qq[r]=q2[0]+q2[1]; }
    #pragma unroll
    for(int r=0;r<2;r++){
      const int s=mi*16+2*w+r;
      const f32x2 rs=bc2(__builtin_amdgcn_rsqf(dpp_sum(qq[r])*(1.0f/D)+1e-5f));
      u32x4 o0,o1;
      #pragma unroll
      for(int i=0;i<4;i++){ o0[i]=cvtpk(v[r][i]*rs); o1[i]=cvtpk(v[r][4+i]*rs); }
      st_sc1(rH,(unsigned)(s*2048+l*16),o0); st_sc1(rH,(unsigned)(s*2048+1024+l*16),o1);
      if(l==0) __builtin_amdgcn_raw_buffer_store_b32(__float_as_uint(__builtin_amdgcn_rcpf(1.0f+__builtin_amdgcn_exp2f(-1.4426950408889634f*z[r]))), rG, (unsigned)(s*4), 0, 16);
    } }
  tileW(IC(0));
  STAMP(13);
  waitv<6+4>();
  #pragma unroll
  for(int a=0;a<7;a++){ pin(bb[a]);
    const f32x4 b4= kg? (f32x4){0,0,0,0} : (f32x4){bflo(bb[a][0]),bfhi(bb[a][0]),bflo(bb[a][1]),bfhi(bb[a][1])};
    #pragma unroll
    for(int b=0;b<4;b++){ acc[a][b]=b4; pin(acc[a][b]); } }
  waitv<4>();
  STAMP(14);
  const unsigned epoch=rfl(gen)+1u;
  __syncthreads();
  if(tid==0) flag_st(flags+(FLG_LN+mi)*FST, epoch);
  STAMP(11); RSTAMP(31);
  if(w==0){
    const unsigned* f0p=flags+(size_t)(FLG_LN+l)*FST; const unsigned* f1p=f0p+64*FST;
    for(unsigned spins=0; spins<(1u<<22); ++spins){ const unsigned a=flag_ld(f0p), b=flag_ld(f1p); if(__all(a==epoch && b==epoch)) break; __builtin_amdgcn_s_sleep(1); } }
  bar();
  STAMP(12); RSTAMP(19);
  tileH(IC(0)); tileW(IC(1)); tileH(IC(1));
  waitv<6>();
  bar();
  { auto rd2=[&](auto o0,auto o1,u32x4& f,unsigned a){ u32x2 lo,hi; ds_tr<decltype(o0)::value>(lo,a); ds_tr<decltype(o1)::value>(hi,a); f=cat(lo,hi); };
    rd2(IC(0),IC(1024),fH[0][0],c.bH[0]); rd2(IC(0),IC(1024),fH[0][1],c.bH[1]); rd2(IC(0),IC(1024),fH[0][2],c.bH[2]); rd2(IC(0),IC(1024),fH[0][3],c.bH[3]);
    rd2(IC(0),IC(4*448),fW[0][0],c.bW[0][0]); rd2(IC(0),IC(4*448),fW[0][1],c.bW[0][1]);
    rd2(IC(64),IC(64+4*448),fW[0][2],c.bW[0][0]); rd2(IC(64),IC(64+4*448),fW[0][3],c.bW[0][1]);
    rd2(IC(128),IC(128+4*448),fW[0][4],c.bW[0][0]); rd2(IC(128),IC(128+4*448),fW[0][5],c.bW[0][1]);
    rd2(IC(0),IC(4*448),fW[0][6],c.bW6[0]);
    waitl<0>(); }
  tileW(IC(2)); tileH(IC(2)); tileW(IC(3));
  STAMP(2); RSTAMP(26);
  for(int t=0;t<24;t+=12){
    it12<0,true,true,10>(c,acc,fH,fW); it12<1,true,true,10>(c,acc,fH,fW); it12<2,true,true,10>(c,acc,fH,fW); it12<3,true,true,10>(c,acc,fH,fW);
    it12<4,true,true,10>(c,acc,fH,fW); it12<5,true,true,10>(c,acc,fH,fW); it12<6,true,true,10>(c,acc,fH,fW); it12<7,true,true,10>(c,acc,fH,fW);
    it12<8,true,true,10>(c,acc,fH,fW); it12<9,true,true,10>(c,acc,fH,fW); it12<10,true,true,10>(c,acc,fH,fW); it12<11,true,true,10>(c,acc,fH,fW); }
  it12<0,true,true,10>(c,acc,fH,fW); it12<1,true,true,10>(c,acc,fH,fW); it12<2,true,true,10>(c,acc,fH,fW);
  it12<3,true,true,10>(c,acc,fH,fW);
  it12<4,true,false,10>(c,acc,fH,fW);
  it12<5,false,false,6>(c,acc,fH,fW);
  it12<6,false,false,0>(c,acc,fH,fW);
  it12<7,false,false,-1,false>(c,acc,fH,fW);
  asm volatile("s_nop 7\n\ts_nop 7" ::: "memory");
  #pragma unroll
  for(int a=0;a<7;a++)
  #pragma unroll
  for(int b=0;b<4;b++) pin(acc[a][b]);
  STAMP(3); RSTAMP(27);
  { const i32x4 srd2=make_srd(p.w2,F*S*2); const int lo_=otid()&63; const unsigned v16=(lo_<16)? (unsigned)(((mi*8+w)*16+lo_)*128) : 0x80000000u;
    asm volatile("s_nop 4");
    touch<0>(dT,v16,srd2,5u*16384*128); touch<0>(dT,v16,srd2,6u*16384*128); }
  bar();
  if(kg==0) epilogue<0>(p,(LAS char*)smem,acc,w,dt,ft); else epilogue<1>(p,(LAS char*)smem,acc,w,dt,ft);
  STAMP(4);
  asm volatile("s_waitcnt vmcnt(7)" : "+v"(dT) :: "memory");
  return epoch;
}
}

namespace fin {
constexpr int PART=16384, SLOT=2*PART, NS=4, GB_OFF=NS*SLOT, LDS_BYTES=GB_OFF+2048;
#define NB0X 16
constexpr int NB0=NB0X;
struct Src { i32x4 srd; unsigned v[2]; unsigned soff; };
struct Bases { unsigned aR[2], aT[2][4]; };
template<int SET,int SL,bool READ,bool RPA,int DMAK  ,int VM>
DI void iter(const Bases& RB, unsigned mS, unsigned mD, Src& sS, Src& sD, f32x4 (&acc)[4][4], u32x4 (&fS)[2][4], u32x4 (&fD)[2][4]){
  constexpr int NX=SET^1, RS=(SL+1)&3, DS=SL;
  constexpr int RHI=RS>>1, RO=(RS&1)*SLOT;
  if constexpr(VM>=0){ waitv<VM>(); bar(); }
  u32x2 tl[4],th[4];
  auto RR=[&](auto ic){ constexpr int i=decltype(ic)::value;
    if constexpr(READ){ if constexpr(RPA) ds_b128<RO+i*2048>(fS[NX][i],RB.aR[RHI]); else ds_b128<RO+PART+i*2048>(fD[NX][i],RB.aR[RHI]); } };
  auto RT=[&](auto ic,auto hc){ constexpr int i=decltype(ic)::value,h=decltype(hc)::value;
    if constexpr(READ){
      if constexpr(RPA){ if constexpr(h==0) ds_tr<RO+PART>(tl[i],RB.aT[RHI][i]); else { ds_tr<RO+PART+1024>(th[i],RB.aT[RHI][i]); fD[NX][i]=cat(tl[i],th[i]); } }
      else { if constexpr(h==0) ds_tr<RO>(tl[i],RB.aT[RHI][i]); else { ds_tr<RO+1024>(th[i],RB.aT[RHI][i]); fS[NX][i]=cat(tl[i],th[i]); } } } };
  auto MS=[&](){ if constexpr(DMAK!=0) set_m0<DS*SLOT>(mS); };
  auto MD=[&](){ if constexpr(DMAK!=0) set_m0<DS*SLOT>(mD); };
  auto DSs=[&](auto kc){ constexpr int k=decltype(kc)::value; if constexpr(DMAK!=0) dma<k*1024,(DMAK==2)?0:2>(sS.v[k],sS.srd,sS.soff); };
  auto DDd=[&](auto kc){ constexpr int k=decltype(kc)::value; if constexpr(DMAK!=0) dma<k*1024,(DMAK==1)?0:2>(sD.v[k],sD.srd,sD.soff); };
  auto MM=[&](auto ac,auto bc){ constexpr int a=decltype(ac)::value,b=decltype(bc)::value; mfma(acc[a][b],fD[SET][a],fS[SET][b]); };
  auto M1=[&](){ if constexpr(DMAK==1) MS(); else MD(); };
  auto M2=[&](){ if constexpr(DMAK==1) MD(); else MS(); };
  auto D1=[&](auto kc){ if constexpr(DMAK==1) DSs(kc); else DDd(kc); };
  auto D2=[&](auto kc){ if constexpr(DMAK==1) DDd(kc); else DSs(kc); };
  MM(IC(0),IC(0)); RR(IC(0)); M1();
  MM(IC(0),IC(1)); RT(IC(0),IC(0)); D1(IC(0));
  MM(IC(0),IC(2)); RT(IC(0),IC(1));
  MM(IC(0),IC(3)); RR(IC(1)); D1(IC(1));
  MM(IC(1),IC(0)); RT(IC(1),IC(0)); M2();
  MM(IC(1),IC(1)); RT(IC(1),IC(1)); D2(IC(0));
  MM(IC(1),IC(2)); RR(IC(2));
  MM(IC(1),IC(3)); RT(IC(2),IC(0)); D2(IC(1));
  MM(IC(2),IC(0)); RT(IC(2),IC(1));
  MM(IC(2),IC(1)); RR(IC(3));
  if constexpr(DMAK==1){ saddi<262144>(sS.soff); saddi<64*2048>(sD.soff); }
  if constexpr(DMAK==2){ saddi<64*4096>(sS.soff); saddi<16384>(sD.soff); }
  MM(IC(2),IC(2)); RT(IC(3),IC(0));
  MM(IC(2),IC(3)); RT(IC(3),IC(1));
  MM(IC(3),IC(0)); MM(IC(3),IC(1)); MM(IC(3),IC(2)); MM(IC(3),IC(3));
  if constexpr(READ) waitl<0>();
}
template<bool PA> DI void mk_bases(Bases& B, unsigned lds0, int wsd, int wd, int kg){
  const int l=otid()&63, g=l>>4,q=(l&15)>>2,pp=l&3,i16=l&15;
  #pragma unroll
  for(int hi=0;hi<2;hi++){
    const unsigned base=lds0+hi*2*SLOT;
    B.aR[hi]=base+(64*(PA?wsd:wd)+i16)*128+(((4*kg+g)^((i16>>1)&7))*16);
    const int row=32*kg+8*g+q; const int sw=q|((g&1)<<2);
    #pragma unroll
    for(int e=0;e<4;e++) B.aT[hi][e]=base+row*256+(((4*(PA?wd:wsd)+e)^sw)*32)+8*pp;
  }
}
template<bool PA> DI void mk_src(const Params& p, Src& sS, Src& sD, int w, int s0, int dt, int half){
  const int l=otid()&63;
  #pragma unroll
  for(int k=0;k<2;k++){
    const int rowR=16*w+8*k+(l>>3); const int chR=(l&7)^((rowR>>1)&7);
    const int rowT=8*w+4*k+(l>>4); const int sw=(rowT&3)|(((rowT>>3)&1)<<2); const int pos=l&15; const int chT=(((pos>>1)^sw)<<1)|(pos&1);
    if constexpr(PA){ sS.v[k]=(chR>>2)*131072+(s0+rowR)*64+(chR&3)*16-1024*k; sD.v[k]=rowT*2048+dt*256+chT*16-1024*k; }
    else            { sS.v[k]=rowT*4096+s0*2+chT*16-1024*k; sD.v[k]=dt*112*8192+(chR>>2)*8192+rowR*64+(chR&3)*16-1024*k; }
  }
  if constexpr(PA){ sS.srd=make_srd(p.ws+WS_O,S*D*2); sD.srd=make_srd(p.wo,D*D*2); sS.soff=0; sD.soff=0; }
  else { sS.srd=make_srd(p.w2,F*S*2); sD.srd=make_srd(p.ws+WS_U,D*F*2); sS.soff=rfl(half? NB0*64*4096 : 0); sD.soff=rfl(half? NB0*16384 : 0); }
}
template<int HALF>
DI void run(const Params& p, char* smem, int st, int dt, unsigned epoch, int myflag, unsigned long long* stamps){
  const unsigned lds0=(unsigned)(size_t)(LAS char*)smem;
  LAS char* sm=(LAS char*)smem;
  const int w=rfl(threadIdx.x>>6);
  const int wsd=w&1, wd=(w>>1)&1, kg=w>>2;
  const int s0=st*128, d0=dt*128;
  const unsigned mS=rfl(lds0+w*2048), mD=rfl(lds0+PART+w*2048);
  unsigned* flags=(unsigned*)(p.ws+WS_FLG);
  f32x4 accA[4][4], accB[4][4]; u32x4 fS[2][4], fD[2][4];
  #pragma unroll
  for(int a=0;a<4;a++)
  #pragma unroll
  for(int b=0;b<4;b++){ accB[a][b]=(f32x4){0,0,0,0}; pin(accB[a][b]); if constexpr(HALF==0){ accA[a][b]=(f32x4){0,0,0,0}; pin(accA[a][b]); } }
  STAMP(20);
  auto load_gb=[&](){
    if(w==0){ const int l=otid()&63; i32x4 sg=make_srd(p.ws+WS_G,S*4); set_m0<0>(rfl(lds0+GB_OFF)); asm volatile("s_nop 4"); dma<0,2>((unsigned)(s0*4+l*16),sg,0u); }
    if(w==1){ const int l=otid()&63; i32x4 sb=make_srd(p.b2,S*2); set_m0<0>(rfl(lds0+GB_OFF+1024)); asm volatile("s_nop 4"); dma<0>((unsigned)(s0*2+l*16),sb,0u); } };
  auto tileS=[&](auto slc, auto pac, Src& sS){ constexpr int SLT=decltype(slc)::value; constexpr bool PA=decltype(pac)::value!=0;
    set_m0<SLT*SLOT>(mS); nop1(); dma<0,PA?2:0>(sS.v[0],sS.srd,sS.soff); dma<1024,PA?2:0>(sS.v[1],sS.srd,sS.soff);
    if constexpr(PA) saddi<262144>(sS.soff); else saddi<64*4096>(sS.soff); };
  auto tileD=[&](auto slc, auto pac, Src& sD){ constexpr int SLT=decltype(slc)::value; constexpr bool PA=decltype(pac)::value!=0;
    set_m0<SLT*SLOT>(mD); nop1(); dma<0,PA?0:2>(sD.v[0],sD.srd,sD.soff); dma<1024,PA?0:2>(sD.v[1],sD.srd,sD.soff);
    if constexpr(PA) saddi<64*2048>(sD.soff); else saddi<16384>(sD.soff); };
  auto first_read=[&](auto pac, const Bases& B){ constexpr bool PA=decltype(pac)::value!=0;
    u32x2 l0,h0,l1,h1,l2,h2,l3,h3;
    if constexpr(PA){
      ds_b128<0>(fS[0][0],B.aR[0]); ds_b128<2048>(fS[0][1],B.aR[0]); ds_b128<4096>(fS[0][2],B.aR[0]); ds_b128<6144>(fS[0][3],B.aR[0]);
      ds_tr<PART>(l0,B.aT[0][0]); ds_tr<PART+1024>(h0,B.aT[0][0]); ds_tr<PART>(l1,B.aT[0][1]); ds_tr<PART+1024>(h1,B.aT[0][1]);
      ds_tr<PART>(l2,B.aT[0][2]); ds_tr<PART+1024>(h2,B.aT[0][2]); ds_tr<PART>(l3,B.aT[0][3]); ds_tr<PART+1024>(h3,B.aT[0][3]);
      fD[0][0]=cat(l0,h0); fD[0][1]=cat(l1,h1); fD[0][2]=cat(l2,h2); fD[0][3]=cat(l3,h3);
    } else {
      ds_b128<PART>(fD[0][0],B.aR[0]); ds_b128<PART+2048>(fD[0][1],B.aR[0]); ds_b128<PART+4096>(fD[0][2],B.aR[0]); ds_b128<PART+6144>(fD[0][3],B.aR[0]);
      ds_tr<0>(l0,B.aT[0][0]); ds_tr<1024>(h0,B.aT[0][0]); ds_tr<0>(l1,B.aT[0][1]); ds_tr<1024>(h1,B.aT[0][1]);
      ds_tr<0>(l2,B.aT[0][2]); ds_tr<1024>(h2,B.aT[0][2]); ds_tr<0>(l3,B.aT[0][3]); ds_tr<1024>(h3,B.aT[0][3]);
      fS[0][0]=cat(l0,h0); fS[0][1]=cat(l1,h1); fS[0][2]=cat(l2,h2); fS[0][3]=cat(l3,h3);
    }
    waitl<0>(); };
  #define ITB(SET,SL,RD,DK,VM) iter<SET,SL,RD,false,DK,VM>(Bb,mS,mD,bS,bD,accB,fS,fD)
  Bases Bb; Src bS,bD;
  mk_src<false>(p,bS,bD,w,s0,dt,HALF); mk_bases<false>(Bb,lds0,wsd,wd,kg);
  waitv<0>(); bar();
  if(threadIdx.x==0) flag_st(flags+myflag*FST, epoch);
  STAMP(18);
  tileS(IC(0),IC(0),bS); tileS(IC(1),IC(0),bS); tileS(IC(2),IC(0),bS); tileS(IC(3),IC(0),bS);
  if constexpr(HALF==0) poll_flags(flags, FLG_U+dt*16, (NB0*64+223)/224, epoch, otid()&63);
  else poll_flags(flags, FLG_U+dt*16+(NB0*64)/224, 16-(NB0*64)/224, epoch, otid()&63);
  STAMP(21);
  load_gb();
  tileD(IC(0),IC(0),bD); tileD(IC(1),IC(0),bD); tileD(IC(2),IC(0),bD); tileD(IC(3),IC(0),bD);
  waitv<2>(); bar(); first_read(IC(0),Bb);
  STAMP(22); RSTAMP(29);
  { constexpr int n=HALF? 56-NB0-4 : NB0-4;
    for(int t=0;t<n;t+=4){ ITB(0,0,true,2,8); ITB(1,1,true,2,8); ITB(0,2,true,2,8); ITB(1,3,true,2,8); } }
  if constexpr(HALF==0){
    Bases Ba; Src aS,aD;
    mk_src<true>(p,aS,aD,w,s0,dt,0);
    poll_flags(flags, FLG_O+(st>>1)*16, 16, epoch, otid()&63);
    iter<0,0,true,false,1,8>(Bb,mS,mD,aS,aD,accB,fS,fD); iter<1,1,true,false,1,8>(Bb,mS,mD,aS,aD,accB,fS,fD); iter<0,2,true,false,1,8>(Bb,mS,mD,aS,aD,accB,fS,fD);
    mk_bases<true>(Ba,lds0,wsd,wd,kg);
    iter<1,3,true,true,1,8>(Ba,mS,mD,aS,aD,accB,fS,fD);
    for(int t=0;t<12;t+=4){ iter<0,0,true,true,1,8>(Ba,mS,mD,aS,aD,accA,fS,fD); iter<1,1,true,true,1,8>(Ba,mS,mD,aS,aD,accA,fS,fD);
                            iter<0,2,true,true,1,8>(Ba,mS,mD,aS,aD,accA,fS,fD); iter<1,3,true,true,1,8>(Ba,mS,mD,aS,aD,accA,fS,fD); }
    iter<0,0,true,true,0,8>(Ba,mS,mD,aS,aD,accA,fS,fD); iter<1,1,true,true,0,4>(Ba,mS,mD,aS,aD,accA,fS,fD); iter<0,2,true,true,0,0>(Ba,mS,mD,aS,aD,accA,fS,fD);
    iter<1,3,false,true,0,-1>(Ba,mS,mD,aS,aD,accA,fS,fD);
  } else {
    ITB(0,0,true,0,8); ITB(1,1,true,0,4); ITB(0,2,true,0,0);
  }
  unsigned xf=0;
  if constexpr(HALF==1){ gld32_sc1(xf, flags+(FLG_X+st*8+dt)*FST);
    ITB(1,3,false,0,-1); }
  asm volatile("s_nop 7\n\ts_nop 7" ::: "memory");
  #pragma unroll
  for(int a=0;a<4;a++)
  #pragma unroll
  for(int b=0;b<4;b++){ pin(accB[a][b]); if constexpr(HALF==0) pin(accA[a][b]); }
  STAMP(23); RSTAMP(30);
  const int tid=otid(), l=tid&63, g=l>>4, i16=l&15;
  const __amdgpu_buffer_rsrc_t rX=make_rsrc(p.ws+WS_XCH,128*32768);
  const unsigned xoff=(unsigned)((st*8+dt)*32768+w*4096+l*16);
  u32x4 t[4];
  if constexpr(HALF==1){
    asm volatile("s_waitcnt vmcnt(0)" : "+v"(xf) :: "memory");
    if(!__all(xf==epoch)) poll_flags(flags, FLG_X+st*8+dt, 1, epoch, l);
    #pragma unroll
    for(int b=0;b<4;b++) t[b]=ld_sc1(rX,xoff+b*1024);
  }
  float gt[4], bz[4];
  #pragma unroll
  for(int b=0;b<4;b++){ const int sl=64*wsd+16*b+i16; gt[b]=*(LAS float*)(sm+GB_OFF+sl*4); bz[b]=__uint_as_float(((unsigned)*(LAS u16*)(sm+GB_OFF+1024+sl*2))<<16); }
  #pragma unroll
  for(int a=0;a<4;a++)
  #pragma unroll
  for(int b=0;b<4;b++){ if constexpr(HALF==0) accB[a][b]=accA[a][b]*gt[b]+accB[a][b]*(1.0f-gt[b]); else accB[a][b]=accB[a][b]*(1.0f-gt[b]); }
  bar();
  { LAS char* wb=sm+w*8192+l*16;
    if(kg==0){
      #pragma unroll
      for(int b=0;b<4;b++){ *(LAS f32x4*)(wb+(b*2+0)*1024)=accB[2][b]; *(LAS f32x4*)(wb+(b*2+1)*1024)=accB[3][b]; }
    } else {
      #pragma unroll
      for(int b=0;b<4;b++){ *(LAS f32x4*)(wb+(b*2+0)*1024)=accB[0][b]; *(LAS f32x4*)(wb+(b*2+1)*1024)=accB[1][b]; }
    } }
  __syncthreads();
  f32x4 r[2][4];
  { LAS char* rb=sm+(w^4)*8192+l*16;
    if(kg==0){
      #pragma unroll
      for(int b=0;b<4;b++){ r[0][b]=accB[0][b]+*(LAS f32x4*)(rb+(b*2+0)*1024); r[1][b]=accB[1][b]+*(LAS f32x4*)(rb+(b*2+1)*1024); }
    } else {
      #pragma unroll
      for(int b=0;b<4;b++){ r[0][b]=accB[2][b]+*(LAS f32x4*)(rb+(b*2+0)*1024); r[1][b]=accB[3][b]+*(LAS f32x4*)(rb+(b*2+1)*1024); }
    } }
  STAMP(24);
  if constexpr(HALF==0){
    #pragma unroll
    for(int b=0;b<4;b++) st_sc1(rX,xoff+b*1024,(u32x4){cvtpk(lo2(r[0][b])),cvtpk(hi2(r[0][b])),cvtpk(lo2(r[1][b])),cvtpk(hi2(r[1][b]))});
    asm volatile("s_waitcnt vmcnt(0)" ::: "memory");
    __syncthreads();
    if(tid==0) flag_st(flags+(FLG_X+st*8+dt)*FST, epoch);
  } else {
    const int offg=(g&1)*16+(g>>1)*8;
    #pragma unroll
    for(int b=0;b<4;b++){
      const float add=(1.0f-gt[b])*bz[b];
      const f32x4 x0=r[0][b]+(f32x4){bflo(t[b][0]),bfhi(t[b][0]),bflo(t[b][1]),bfhi(t[b][1])}+add, x1=r[1][b]+(f32x4){bflo(t[b][2]),bfhi(t[b][2]),bflo(t[b][3]),bfhi(t[b][3])}+add;
      const pr2 r0=sw16(cvtpk(lo2(x0)),cvtpk(lo2(x1))), r1=sw16(cvtpk(hi2(x0)),cvtpk(hi2(x1)));
      const u32x4 ov={r0[0],r1[0],r0[1],r1[1]};
      const int s=s0+64*wsd+16*b+i16;
      *(u32x4*)(p.out+(size_t)s*D+d0+64*wd+32*kg+offg)=ov;
    }
  }
  STAMP(25);
}
}

#define MAP 4
__global__ __launch_bounds__(512,2) void k_fused(Params p){
  extern __shared__ __attribute__((aligned(16))) char smem[];
  unsigned long long* stamps=(unsigned long long*)(p.ws+WS_STM);
  (void)stamps;
  RSTAMP(0); STAMP(17);
  const int b=blockIdx.x, xcd=b&7, j=b>>3;
  const int jj=j>>1;
  const bool isq=(j&1)==0; const int pc=2*xcd+(jj>>3), pr=jj&7, mi=xcd*16+jj;
  unsigned epoch; int myflag;
  if(isq){ epoch=qkv::run(p,smem,pr,pc,stamps); myflag=FLG_O+pr*16+pc; } else { epoch=mix1::run(p,smem,pr,pc,mi,stamps); myflag=FLG_U+pr*16+pc; }
  RSTAMP(16);
  const int st=((xcd&3)&1)*8+(j&7), dtf=((xcd&3)>>1)*4+(j>>3);
  const bool h0=xcd<4;
  if(h0) fin::run<0>(p,smem,st,dtf,epoch,myflag,stamps); else fin::run<1>(p,smem,st,dtf,epoch,myflag,stamps);
  if(b==0 && threadIdx.x==0) flag_st((unsigned*)(p.ws+WS_FLG), epoch);
  RSTAMP(15);
}

extern "C" void kernel_launch(void* const* d_in, const int* in_sizes, int n_in, void* d_out, int out_size, void* d_ws, size_t ws_size, hipStream_t stream){
  (void)in_sizes; (void)n_in; (void)out_size; (void)ws_size;
  Params p;
  p.x=(const u16*)d_in[0]; p.wqkv=(const u16*)d_in[1]; p.wphi=(const u16*)d_in[2]; p.wo=(const u16*)d_in[3];
  p.w1=(const u16*)d_in[5]; p.b1=(const u16*)d_in[6]; p.w2=(const u16*)d_in[7]; p.b2=(const u16*)d_in[8]; p.gw=(const u16*)d_in[9];
  p.out=(u16*)d_out; p.ws=(char*)d_ws;
  constexpr int L1_=(qkv::LDS_BYTES>mix1::LDS_BYTES)?qkv::LDS_BYTES:mix1::LDS_BYTES;
  constexpr int LDS_ALL=(L1_>fin::LDS_BYTES)?L1_:fin::LDS_BYTES;
  (void)hipFuncSetAttribute((const void*)k_fused, hipFuncAttributeMaxDynamicSharedMemorySize, LDS_ALL);
  k_fused<<<256,512,LDS_ALL,stream>>>(p);
}
```

```cpp
#include <hip/hip_runtime.h>
#include <hip/hip_bf16.h>
#include <type_traits>
#include <utility>

#define LAS __attribute__((address_space(3)))
#define DI __device__ __forceinline__
#define IC(n) std::integral_constant<int,(n)>{}
typedef unsigned short u16;
typedef float f32x4 __attribute__((ext_vector_type(4)));
typedef float f32x2 __attribute__((ext_vector_type(2)));
typedef int i32x4 __attribute__((ext_vector_type(4)));
typedef unsigned u32x4 __attribute__((ext_vector_type(4)));
typedef unsigned u32x2 __attribute__((ext_vector_type(2)));
typedef short bf16x8 __attribute__((ext_vector_type(8)));
typedef __bf16 bf16x2_t __attribute__((ext_vector_type(2)));

constexpr int S=2048, D=1024, F=3584;
constexpr size_t WS_H   = 0;
constexpr size_t WS_U   = WS_H + (size_t)S*D*2;
constexpr size_t WS_O   = WS_U + (size_t)D*F*2;
constexpr size_t WS_KVP = WS_O + (size_t)S*D*2;
constexpr size_t WS_ZP  = WS_KVP + (size_t)16*8*8*1024;
constexpr size_t WS_G   = WS_ZP + (size_t)16*8*2*64*4;
constexpr size_t WS_XCH = WS_G + 8192;
constexpr size_t WS_FLG = WS_XCH + (size_t)128*65536;
constexpr size_t WS_STM = WS_FLG + 131072;
#define FST 32
constexpr int FLG_LN = 64;
constexpr int FLG_KV = 192;
constexpr int FLG_X  = 320;
constexpr int PFLG_O = 16384;
constexpr int PFLG_U = 16384+512;

template<int OFF> DI void ds_tr(u32x2& d, unsigned a){ asm volatile("ds_read_b64_tr_b16 %0, %1 offset:%2" : "=v"(d) : "v"(a), "i"(OFF)); }
template<int OFF> DI void ds_b128(u32x4& d, unsigned a){ asm volatile("ds_read_b128 %0, %1 offset:%2" : "=v"(d) : "v"(a), "i"(OFF)); }
DI void mfma(f32x4& c, const u32x4& a, const u32x4& b){ asm volatile("v_mfma_f32_16x16x32_bf16 %0, %1, %2, %0" : "+v"(c) : "v"(a), "v"(b)); }
DI f32x4 mfma_b(const u32x4& a, const u32x4& b, const f32x4& c){ return __builtin_amdgcn_mfma_f32_16x16x32_bf16(__builtin_bit_cast(bf16x8,a), __builtin_bit_cast(bf16x8,b), c, 0,0,0); }
template<int IMM,int POL=0> DI void dma(unsigned voff, const i32x4& srd, unsigned soff){
  if constexpr(POL==2) asm volatile("buffer_load_dwordx4 %0, %1, %2 offen offset:%3 sc1 lds" :: "v"(voff), "s"(srd), "s"(soff), "i"(IMM) : "memory");
  else if constexpr(POL==1) asm volatile("buffer_load_dwordx4 %0, %1, %2 offen offset:%3 nt lds" :: "v"(voff), "s"(srd), "s"(soff), "i"(IMM) : "memory");
  else asm volatile("buffer_load_dwordx4 %0, %1, %2 offen offset:%3 lds" :: "v"(voff), "s"(srd), "s"(soff), "i"(IMM) : "memory"); }
template<int IMM> DI void touch(unsigned& d, unsigned voff, const i32x4& srd, unsigned soff){
  asm volatile("buffer_load_dword %0, %1, %2, %3 offen offset:%4" : "+v"(d) : "v"(voff), "s"(srd), "s"(soff), "i"(IMM) : "memory"); }
template<int V> DI void set_m0(unsigned base){ asm volatile("s_add_u32 m0, %0, %1" :: "s"(base), "i"(V) : "scc"); }
DI void nop1(){ asm volatile("s_nop 0"); }
template<int N> DI void waitv(){ asm volatile("s_waitcnt vmcnt(%0)" :: "n"(N) : "memory"); }
template<int N> DI void waitl(){ asm volatile("s_waitcnt lgkmcnt(%0)" :: "n"(N) : "memory"); }
DI void bar(){ asm volatile("s_barrier" ::: "memory"); }
template<int V> DI void saddi(unsigned& x){ asm volatile("s_add_u32 %0, %0, %1" : "+s"(x) : "i"(V) : "scc"); }
DI void pin(f32x4& x){ asm volatile("" : "+v"(x)); }
DI void pin(u32x4& x){ asm volatile("" : "+v"(x)); }
DI void pin(u32x2& x){ asm volatile("" : "+v"(x)); }
DI void pin(unsigned& x){ asm volatile("" : "+v"(x)); }
DI u32x4 cat(u32x2 lo,u32x2 hi){ return __builtin_shufflevector(lo,hi,0,1,2,3); }
DI unsigned cvtpk(float lo,float hi){ f32x2 v={lo,hi}; bf16x2_t b=__builtin_convertvector(v,bf16x2_t); return __builtin_bit_cast(unsigned,b); }
DI unsigned cvtpk(f32x2 v){ bf16x2_t b=__builtin_convertvector(v,bf16x2_t); return __builtin_bit_cast(unsigned,b); }
DI float bflo(unsigned x){ return __uint_as_float(x<<16); }
DI float bfhi(unsigned x){ return __uint_as_float(x&0xffff0000u); }
DI f32x2 bf2(unsigned x){ return (f32x2){bflo(x),bfhi(x)}; }
DI f32x2 lo2(const f32x4& v){ return __builtin_shufflevector(v,v,0,1); }
DI f32x2 hi2(const f32x4& v){ return __builtin_shufflevector(v,v,2,3); }
DI f32x2 fma2(f32x2 a,f32x2 b,f32x2 c){ return __builtin_elementwise_fma(a,b,c); }
DI f32x2 bc2(float x){ return (f32x2){x,x}; }
DI int otid(){ int t=threadIdx.x; asm volatile("" : "+v"(t)); return t; }
DI unsigned rfl(unsigned x){ return __builtin_amdgcn_readfirstlane(x); }
DI i32x4 make_srd(const void* p, unsigned bytes){
  unsigned long long a=(unsigned long long)p; i32x4 s;
  s[0]=(int)rfl((unsigned)a); s[1]=(int)(rfl((unsigned)(a>>32))&0xffff); s[2]=(int)bytes; s[3]=0x00020000; return s; }
DI __amdgpu_buffer_rsrc_t make_rsrc(const void* p, unsigned bytes){ return __builtin_amdgcn_make_buffer_rsrc((void*)p, 0, bytes, 0x00020000); }
DI void st_sc1(__amdgpu_buffer_rsrc_t r, unsigned off, const u32x4& v){ __builtin_amdgcn_raw_buffer_store_b128(v, r, off, 0, 16); }
DI u32x4 ld_sc1(__amdgpu_buffer_rsrc_t r, unsigned off){ return __builtin_amdgcn_raw_buffer_load_b128(r, off, 0, 16); }
template<int OFF> DI void gld128(u32x4& d, const void* p){ asm volatile("global_load_dwordx4 %0, %1, off offset:%2" : "=v"(d) : "v"(p), "i"(OFF) : "memory"); }
template<int OFF> DI void gld64(u32x2& d, const void* p){ asm volatile("global_load_dwordx2 %0, %1, off offset:%2" : "=v"(d) : "v"(p), "i"(OFF) : "memory"); }
DI void gld32_sc1(unsigned& d, const void* p){ asm volatile("global_load_dword %0, %1, off sc1" : "=v"(d) : "v"(p) : "memory"); }
typedef unsigned pr2 __attribute__((ext_vector_type(2)));
DI pr2 sw16(unsigned a,unsigned b){ auto r=__builtin_amdgcn_permlane16_swap(a,b,false,false); return (pr2){r[0],r[1]}; }
DI pr2 sw32(unsigned a,unsigned b){ auto r=__builtin_amdgcn_permlane32_swap(a,b,false,false); return (pr2){r[0],r[1]}; }
DI float mx2(float a,float b){ return __builtin_elementwise_maximum(a,b); }
DI float mx8(const f32x4& a,const f32x4& b){ return mx2(mx2(mx2(mx2(a[0],a[1]),a[2]),mx2(mx2(a[3],b[0]),b[1])),mx2(b[2],b[3])); }
DI float mn2(float a,float b){ return __builtin_elementwise_minimum(a,b); }
DI float mn8(const f32x4& a,const f32x4& b){ return mn2(mn2(mn2(mn2(a[0],a[1]),a[2]),mn2(mn2(a[3],b[0]),b[1])),mn2(b[2],b[3])); }
DI void xr_max2(float& a, float& b){
  pr2 r=sw32(__float_as_uint(a),__float_as_uint(b)); float t=mx2(__uint_as_float(r[0]),__uint_as_float(r[1]));
  r=sw16(__float_as_uint(t),__float_as_uint(t)); t=mx2(__uint_as_float(r[0]),__uint_as_float(r[1]));
  r=sw32(__float_as_uint(t),__float_as_uint(t)); a=__uint_as_float(r[0]); b=__uint_as_float(r[1]); }
DI unsigned flag_ld(const unsigned* p){ return __hip_atomic_load(p, __ATOMIC_RELAXED, __HIP_MEMORY_SCOPE_AGENT); }
DI void flag_st(unsigned* p, unsigned v){ __hip_atomic_store(p, v, __ATOMIC_RELAXED, __HIP_MEMORY_SCOPE_AGENT); }
DI void poll_flags(const unsigned* flags, int first, int n, unsigned epoch, int lane){
  const unsigned* p = flags + (size_t)(first + (lane<n?lane:0))*FST;
  for(unsigned spins=0; spins<(1u<<22); ++spins){
    unsigned v=flag_ld(p);
    if(__all(v==epoch)) break;
    __builtin_amdgcn_s_sleep(1);
  }
}
DI void poll_line(const unsigned* line, unsigned mask, unsigned epoch, int lane){
  const unsigned* p = line + (lane&15)*2;
  for(unsigned spins=0; spins<(1u<<22); ++spins){
    const unsigned v=flag_ld(p);
    if(((unsigned)__ballot(v==epoch)&mask)==mask) break;
    __builtin_amdgcn_s_sleep(1);
  }
}
#define STAMP(i)
#define RSTAMP(i)

struct Params {
  const u16* x; const u16* wqkv; const u16* wphi; const u16* wo; const u16* w1; const u16* b1; const u16* w2; const u16* b2; const u16* gw;
  u16* out; char* ws;
};

#define QLA 2
#define MT0 0
#define EVALL 1
constexpr int EVN=EVALL?7:14;
#define QNT 0
constexpr int TW=QNT?0:1;
namespace qkv {
constexpr int XSLOT=256*128, WSLOT=64*384;
constexpr int XOFF=0, WOFF=2*XSLOT;
constexpr int WPHI_OFF=WOFF+3*WSLOT;
constexpr int LDS_BYTES=WPHI_OFF+4096;
struct Ctx { unsigned aX[2], aW[2], vX[4], vW[3], vT, v7, v16, dT; i32x4 srdX, srdW, srd1, srd2; unsigned mX, mW, soffX, soffW, soff1, soff2;
};
DI void ev_w1(Ctx& c){ touch<0>(c.dT,c.v7,c.srd1,c.soff1); saddi<EVN*1024*128>(c.soff1); }
template<int E> DI void ev_16(Ctx& c){ c.soff2=E*16384*128; touch<0>(c.dT,c.v16,c.srd2,c.soff2); }

template<int SET,int RX,int RW,int RKS,bool READ,bool DOX,int DXS,bool DOW,int DWS,bool TCH,int EV>
DI void step(Ctx& c, f32x4 (&acc)[3][2][4], u32x4 (&fX)[2][4], u32x4 (&fW)[2][3][2]){
  constexpr int NX=SET^1;
  u32x2 wl[3][2], wh[3][2];
  auto RXf=[&](auto mc){ constexpr int m=decltype(mc)::value; if constexpr(READ) ds_b128<RX*XSLOT+m*2048>(fX[NX][m], c.aX[RKS]); };
  auto RWf=[&](auto sc,auto ec,auto tc){ constexpr int s=decltype(sc)::value,e=decltype(ec)::value,t=decltype(tc)::value;
    if constexpr(READ){ constexpr int o=RW*WSLOT+RKS*12288+s*1024;
      if constexpr(t==0) ds_tr<o>(wl[s][e], c.aW[e]); else { ds_tr<o+512>(wh[s][e], c.aW[e]); fW[NX][s][e]=cat(wl[s][e],wh[s][e]); } } };
  auto MX=[&](){ if constexpr(DOX) set_m0<DXS*XSLOT>(c.mX); };
  auto MW=[&](){ if constexpr(DOW) set_m0<DWS*WSLOT>(c.mW); };
  auto DX=[&](auto kc){ constexpr int k=decltype(kc)::value; if constexpr(DOX) dma<k*1024>(c.vX[k], c.srdX, c.soffX); };
  auto DW=[&](auto kc){ constexpr int k=decltype(kc)::value; if constexpr(DOW) dma<k*1024,QNT>(c.vW[k], c.srdW, c.soffW); };
  auto MM=[&](auto sc,auto ec,auto mc){ constexpr int s=decltype(sc)::value,e=decltype(ec)::value,m=decltype(mc)::value;
    if constexpr(s<2) mfma(acc[s][e][m], fW[SET][s][e], fX[SET][m]); else mfma(acc[s][e][m], fX[SET][m], fW[SET][s][e]); };
  MM(IC(0),IC(0),IC(0)); RXf(IC(0)); MX();
  MM(IC(0),IC(0),IC(1)); RWf(IC(0),IC(0),IC(0)); DX(IC(0));
  MM(IC(0),IC(0),IC(2)); RWf(IC(0),IC(0),IC(1));
  MM(IC(0),IC(0),IC(3)); RXf(IC(1)); DX(IC(1));
  MM(IC(0),IC(1),IC(0)); RWf(IC(0),IC(1),IC(0));
  MM(IC(0),IC(1),IC(1)); RWf(IC(0),IC(1),IC(1)); DX(IC(2));
  MM(IC(0),IC(1),IC(2)); RXf(IC(2));
  MM(IC(0),IC(1),IC(3)); RXf(IC(3)); DX(IC(3));
  MM(IC(1),IC(0),IC(0)); RWf(IC(1),IC(0),IC(0)); MW();
  MM(IC(1),IC(0),IC(1)); RWf(IC(1),IC(0),IC(1)); DW(IC(0));
  MM(IC(1),IC(0),IC(2)); RWf(IC(1),IC(1),IC(0));
  MM(IC(1),IC(0),IC(3)); RWf(IC(1),IC(1),IC(1)); DW(IC(1));
  MM(IC(1),IC(1),IC(0)); RWf(IC(2),IC(0),IC(0));
  MM(IC(1),IC(1),IC(1)); RWf(IC(2),IC(0),IC(1)); DW(IC(2));
  MM(IC(1),IC(1),IC(2)); RWf(IC(2),IC(1),IC(0));
  MM(IC(1),IC(1),IC(3)); RWf(IC(2),IC(1),IC(1));
  if constexpr(TCH) touch<0>(c.dT, c.vT, c.srdW, c.soffW);
  MM(IC(2),IC(0),IC(0));
  if constexpr(EV==1) ev_w1(c);
  if constexpr(EV==6){ ev_w1(c); ev_16<0>(c); }
  else if constexpr(EV>=2) ev_16<EV-2>(c);
  if constexpr(DOX) saddi<128>(c.soffX);
  if constexpr(DOW) saddi<64*6144>(c.soffW);
  MM(IC(2),IC(0),IC(1)); MM(IC(2),IC(0),IC(2)); MM(IC(2),IC(0),IC(3));
  MM(IC(2),IC(1),IC(0)); MM(IC(2),IC(1),IC(1)); MM(IC(2),IC(1),IC(2)); MM(IC(2),IC(1),IC(3));
  if constexpr(READ) waitl<0>();
}
template<int T6,int VM,bool DOX,bool DOW,int EV>
DI void iter(Ctx& c, f32x4 (&acc)[3][2][4], u32x4 (&fX)[2][4], u32x4 (&fW)[2][3][2]){
  step<0,T6&1,T6%3,1,true,false,0,false,0,false,0>(c,acc,fX,fW);
  if constexpr(VM>=0){ waitv<VM>(); bar(); }
  step<1,(T6+1)&1,(T6+1)%3,0,(VM>=0),DOX,T6&1,DOW,T6%3,TW&&DOW&&((T6&1)==0),EV>(c,acc,fX,fW);
}

template<int N,int M0>
DI void feat(const u32x4 (&Ap)[2], const f32x4 (&a)[2][4], f32x2 (&ep)[N][4], f32x2 (&en)[N][4]){
  constexpr float L2E=1.4426950408889634f;
  const f32x4 zero={0,0,0,0};
  f32x4 P0[N], P1[N]; float mx[N], nm[N];
  #pragma unroll
  for(int i=0;i<N;i++){ const int m=M0+i;
    u32x4 B; B[0]=cvtpk(lo2(a[0][m])); B[1]=cvtpk(hi2(a[0][m])); B[2]=cvtpk(lo2(a[1][m])); B[3]=cvtpk(hi2(a[1][m]));
    P0[i]=mfma_b(Ap[0],B,zero); P1[i]=mfma_b(Ap[1],B,zero); }
  #pragma unroll
  for(int i=0;i<N;i++){ mx[i]=mx8(P0[i],P1[i]); nm[i]=-mn8(P0[i],P1[i]); }
  pr2 r[N]; float t[N];
  #pragma unroll
  for(int i=0;i<N;i++){ r[i]=sw32(__float_as_uint(mx[i]),__float_as_uint(nm[i])); t[i]=mx2(__uint_as_float(r[i][0]),__uint_as_float(r[i][1])); }
  #pragma unroll
  for(int i=0;i<N;i++){ r[i]=sw16(__float_as_uint(t[i]),__float_as_uint(t[i])); t[i]=mx2(__uint_as_float(r[i][0]),__uint_as_float(r[i][1])); }
  #pragma unroll
  for(int i=0;i<N;i++){ r[i]=sw32(__float_as_uint(t[i]),__float_as_uint(t[i])); }
  #pragma unroll
  for(int i=0;i<N;i++){
    const f32x2 cp=bc2(__uint_as_float(r[i][0])*-L2E), cn=bc2(__uint_as_float(r[i][1])*-L2E), kp=bc2(L2E), kn=bc2(-L2E);
    const f32x2 q[4]={lo2(P0[i]),hi2(P0[i]),lo2(P1[i]),hi2(P1[i])};
    #pragma unroll
    for(int k=0;k<4;k++){
      const f32x2 tp=fma2(q[k],kp,cp), tn=fma2(q[k],kn,cn);
      ep[i][k]=(f32x2){__builtin_amdgcn_exp2f(tp[0]),__builtin_amdgcn_exp2f(tp[1])};
      en[i][k]=(f32x2){__builtin_amdgcn_exp2f(tn[0]),__builtin_amdgcn_exp2f(tn[1])};
    } }
}
DI u32x4 pack8(const f32x2 (&v)[4]){ return (u32x4){cvtpk(v[0]),cvtpk(v[1]),cvtpk(v[2]),cvtpk(v[3])}; }
DI float dotk(unsigned x,unsigned k,float c){ return __builtin_amdgcn_fdot2_f32_bf16(__builtin_bit_cast(bf16x2_t,x),__builtin_bit_cast(bf16x2_t,k),c,false); }

DI void epilogue(const Params& p, char* smem, f32x4 (&acc)[3][2][4], int rt, int ct, int w, unsigned& dT, unsigned& epoch_out, unsigned long long* stamps){
  const unsigned lds0=(unsigned)(size_t)(LAS char*)smem;
  LAS char* sm=(LAS char*)smem;
  const int tid=otid(), l=tid&63;
  const int g=l>>4,q=(l&15)>>2,pp=l&3,i16=l&15;
  const int wr=w&3, wh=w>>2;
  constexpr int TPHI=0, VREG=65536, SHR=98304;
  char* ws=p.ws;
  unsigned* flags=(unsigned*)(ws+WS_FLG);
  unsigned gen; gld32_sc1(gen, flags);
  u32x4 Ap[2];
  { const unsigned b=lds0+WPHI_OFF+wh*2048+(4*g+q)*64+pp*8;
    u32x2 l0,h0,l1,h1; ds_tr<0>(l0,b); ds_tr<1024>(h0,b); ds_tr<32>(l1,b); ds_tr<32+1024>(h1,b);
    #pragma unroll
    for(int mp=0;mp<2;mp++)
    #pragma unroll
    for(int e=0;e<2;e++){
      u32x4 fv; fv[0]=cvtpk(lo2(acc[2][e][2*mp])); fv[1]=cvtpk(hi2(acc[2][e][2*mp])); fv[2]=cvtpk(lo2(acc[2][e][2*mp+1])); fv[3]=cvtpk(hi2(acc[2][e][2*mp+1]));
      *(LAS u32x4*)(sm+VREG+w*4096+(mp*2+e)*1024+l*16)=fv;
    }
    asm volatile("s_waitcnt lgkmcnt(0)" : "+v"(l0),"+v"(h0),"+v"(l1),"+v"(h1) :: "memory");
    Ap[0]=cat(l0,h0); Ap[1]=cat(l1,h1); }
  const u32x4 ones={0x3F803F80u,0x3F803F80u,0x3F803F80u,0x3F803F80u};
  const f32x4 zero={0,0,0,0};
  STAMP(9);
  { const int swp=(((i16>>1)&3)<<2)|(((i16>>3)&1)<<1)|(i16&1);
    f32x2 ep[4][4], en[4][4];
    feat<4,0>(Ap,acc[1],ep,en);
    f32x4 sp[4], sn[4];
    #pragma unroll
    for(int m=0;m<4;m++){ sp[m]=mfma_b(ones,pack8(ep[m]),zero); sn[m]=mfma_b(ones,pack8(en[m]),zero); }
    #pragma unroll
    for(int m=0;m<4;m++){
      const f32x2 rp=bc2(__builtin_amdgcn_rcpf(sp[m][0])), rn=bc2(__builtin_amdgcn_rcpf(sn[m][0]));
      LAS char* row=sm+TPHI+w*8192+(16*m+i16)*128;
      *(LAS u32x2*)(row+(((0+g)^swp)*8))=(u32x2){cvtpk(ep[m][0]*rp),cvtpk(ep[m][1]*rp)};
      *(LAS u32x2*)(row+(((4+g)^swp)*8))=(u32x2){cvtpk(ep[m][2]*rp),cvtpk(ep[m][3]*rp)};
      *(LAS u32x2*)(row+(((8+g)^swp)*8))=(u32x2){cvtpk(en[m][0]*rn),cvtpk(en[m][1]*rn)};
      *(LAS u32x2*)(row+(((12+g)^swp)*8))=(u32x2){cvtpk(en[m][2]*rn),cvtpk(en[m][3]*rn)};
    } }
  STAMP(10);
  __syncthreads();
  STAMP(4);
  f32x4 kv0={0,0,0,0},kv1={0,0,0,0},zz={0,0,0,0};
  { const int r15=4*g+q; const int swr=(((r15>>1)&3)<<2)|(((r15>>3)&1)<<1)|(r15&1);
    const unsigned ab=lds0+TPHI+wh*4*8192+r15*128+(((4*wr+pp)^swr)*8);
    const unsigned vb=lds0+VREG+wh*4*4096+l*16;
    u32x2 a[4][4]; u32x4 v[4][4];
    auto rd=[&](auto sc){ constexpr int s_=decltype(sc)::value;
      ds_tr<s_*8192+0>(a[s_][0],ab); ds_tr<s_*8192+2048>(a[s_][1],ab); ds_tr<s_*8192+4096>(a[s_][2],ab); ds_tr<s_*8192+6144>(a[s_][3],ab);
      ds_b128<s_*4096>(v[s_][0],vb); ds_b128<s_*4096+1024>(v[s_][1],vb); ds_b128<s_*4096+2048>(v[s_][2],vb); ds_b128<s_*4096+3072>(v[s_][3],vb); };
    rd(IC(0)); rd(IC(1)); rd(IC(2)); rd(IC(3));
    waitl<0>();
    #pragma unroll
    for(int s_=0;s_<4;s_++){
      #pragma unroll
      for(int k=0;k<4;k++){ pin(a[s_][k]); pin(v[s_][k]); }
      const u32x4 A0=cat(a[s_][0],a[s_][1]), A1=cat(a[s_][2],a[s_][3]);
      kv0=mfma_b(A0,v[s_][0],kv0); kv1=mfma_b(A0,v[s_][1],kv1); zz=mfma_b(A0,ones,zz);
      kv0=mfma_b(A1,v[s_][2],kv0); kv1=mfma_b(A1,v[s_][3],kv1); zz=mfma_b(A1,ones,zz); }
  }
  const __amdgpu_buffer_rsrc_t rKV=make_rsrc(ws+WS_KVP,16*8*8*1024), rZ=make_rsrc(ws+WS_ZP,16*8*2*64*4);
  st_sc1(rKV,(unsigned)(((ct*8+rt)*8+w)*1024+l*16),(u32x4){cvtpk(lo2(kv0)),cvtpk(hi2(kv0)),cvtpk(lo2(kv1)),cvtpk(hi2(kv1))});
  if(i16==0) st_sc1(rZ,(unsigned)((((ct*8+rt)*2+wh)*64+16*wr+4*g)*4),__builtin_bit_cast(u32x4,zz));
  u32x4 phq[4][2]; float rq[4][2];
  auto qblk=[&](auto nc,auto mc){ constexpr int n=decltype(nc)::value, m0=decltype(mc)::value;
    f32x2 ep[n][4], en[n][4];
    feat<n,m0>(Ap,acc[0],ep,en);
    #pragma unroll
    for(int i=0;i<n;i++){ phq[m0+i][0]=pack8(ep[i]); phq[m0+i][1]=pack8(en[i]); }
    #pragma unroll
    for(int i=0;i<n;i++){ const f32x4 sp=mfma_b(ones,phq[m0+i][0],zero), sn=mfma_b(ones,phq[m0+i][1],zero);
      rq[m0+i][0]=__builtin_amdgcn_rcpf(sp[0]); rq[m0+i][1]=__builtin_amdgcn_rcpf(sn[0]); } };
  qblk(IC(2),IC(0));
  asm volatile("s_waitcnt vmcnt(0)" : "+v"(gen), "+v"(dT) :: "memory");
  const unsigned epoch=rfl(gen)+1u; epoch_out=epoch;
  __syncthreads();
  if(tid==0) flag_st(flags+(FLG_KV+ct*8+rt)*FST, epoch);
  STAMP(5);
  qblk(IC(1),IC(2));
  poll_flags(flags, FLG_KV+ct*8, 8, epoch, l);
  STAMP(6);
  { u32x4 t0[8],tz[8];
    #pragma unroll
    for(int r=0;r<8;r++){ t0[r]=ld_sc1(rKV,(unsigned)(((ct*8+r)*8+w)*1024+l*16)); tz[r]=ld_sc1(rZ,(unsigned)((((ct*8+r)*2+wh)*64+16*wr+4*g)*4)); }
    asm volatile("" ::: "memory");
    qblk(IC(1),IC(3));
    float s[8]={0,0,0,0,0,0,0,0}; f32x4 sz={0,0,0,0};
    unsigned klo=0x00003F80u, khi=0x3F800000u; asm volatile("" : "+v"(klo), "+v"(khi));
    #pragma unroll
    for(int r=0;r<8;r++){
      #pragma unroll
      for(int k=0;k<4;k++){ s[2*k]=dotk(t0[r][k],klo,s[2*k]); s[2*k+1]=dotk(t0[r][k],khi,s[2*k+1]); }
      sz+=__builtin_bit_cast(f32x4,tz[r]); }
    LAS char* sb=sm+SHR+w*1536+l*8;
    *(LAS u32x2*)(sb)=(u32x2){cvtpk(s[0],s[1]),cvtpk(s[2],s[3])};
    *(LAS u32x2*)(sb+512)=(u32x2){cvtpk(s[4],s[5]),cvtpk(s[6],s[7])};
    *(LAS u32x2*)(sb+1024)=(u32x2){cvtpk(lo2(sz)),cvtpk(hi2(sz))};
  }
  __syncthreads();
  STAMP(7);
  { u32x4 kf[2][2], zf[2];
    #pragma unroll
    for(int sg=0;sg<2;sg++){
      LAS char* b0=sm+SHR+(wh*4+2*sg)*1536+l*8; LAS char* b1=b0+1536;
      kf[sg][0]=cat(*(LAS u32x2*)(b0),*(LAS u32x2*)(b1)); kf[sg][1]=cat(*(LAS u32x2*)(b0+512),*(LAS u32x2*)(b1+512)); zf[sg]=cat(*(LAS u32x2*)(b0+1024),*(LAS u32x2*)(b1+1024)); }
    const __amdgpu_buffer_rsrc_t rO=make_rsrc(ws+WS_O,S*D*2);
    const int offg=(g&1)*32+(g>>1)*16;
    f32x4 n0p[4],n0n[4],n1p[4],n1n[4],dp[4],dn[4];
    #pragma unroll
    for(int m=0;m<4;m++){
      dp[m]=mfma_b(zf[0],phq[m][0],zero); dn[m]=mfma_b(zf[1],phq[m][1],zero);
      n0p[m]=mfma_b(kf[0][0],phq[m][0],zero); n0n[m]=mfma_b(kf[1][0],phq[m][1],zero);
      n1p[m]=mfma_b(kf[0][1],phq[m][0],zero); n1n[m]=mfma_b(kf[1][1],phq[m][1],zero); }
    #pragma unroll
    for(int m=0;m<4;m++){
      const float rd=__builtin_amdgcn_rcpf(dp[m][0]*rq[m][0]+dn[m][0]*rq[m][1]+1e-6f);
      const f32x2 a=bc2(rq[m][0]*rd), b=bc2(rq[m][1]*rd);
      const unsigned a0=cvtpk(fma2(lo2(n0p[m]),a,lo2(n0n[m])*b)), a1=cvtpk(fma2(hi2(n0p[m]),a,hi2(n0n[m])*b));
      const unsigned b0=cvtpk(fma2(lo2(n1p[m]),a,lo2(n1n[m])*b)), b1=cvtpk(fma2(hi2(n1p[m]),a,hi2(n1n[m])*b));
      const pr2 r0=sw16(a0,b0), r1=sw16(a1,b1);
      const u32x4 ov={r0[0],r1[0],r0[1],r1[1]};
      const int tok=rt*256+64*wr+16*m+i16;
      st_sc1(rO,(unsigned)((ct*2+wh)*131072+tok*64+offg),ov);
    }
  }
  STAMP(8);
}

DI unsigned run(const Params& p, char* smem, int rt, int ct, unsigned& dC, unsigned long long* stamps){
  const unsigned lds0=(unsigned)(size_t)(LAS char*)smem;
  const int tid=threadIdx.x, w=rfl(tid>>6), l=tid&63;
  const int g=l>>4,q=(l&15)>>2,pp=l&3,i16=l&15;
  const int wr=w&3, wh=w>>2;
  Ctx c; f32x4 acc[3][2][4]; u32x4 fX[2][4], fW[2][3][2];
  c.srdX=make_srd(p.x,S*D*2); c.srdW=make_srd(p.wqkv,D*3*D*2);
  c.mX=rfl(lds0+XOFF+w*4096); c.mW=rfl(lds0+WOFF+w*3072);
  #pragma unroll
  for(int k=0;k<4;k++){ const int row=32*w+8*k+(l>>3); const int ch=(l&7)^((row>>1)&7); c.vX[k]=(rt*256+row)*2048+ch*16-1024*k; }
  #pragma unroll
  for(int s=0;s<3;s++){ const int row=8*w+(l>>3); const int sw=((row>>1)&1)|(((row>>3)&1)<<1); const int pos=l&7; const int ch=(((pos>>1)^sw)<<1)|(pos&1);
    c.vW[s]=row*6144+s*2048+ct*128+ch*16-1024*s; }
  { const int idx=w*6+l, tl=idx/24, rem=idx-tl*24, row=8*rt+rem/3, s=rem-(rem/3)*3;
    c.vT=(l<6)? (unsigned)(((tl+QLA)*64+row)*6144+s*2048+ct*128) : 0x80000000u; c.dT=0; }
  { const int gwv=(ct*8+rt)*8+w;
    c.v7=(l<EVN)? (unsigned)((gwv*EVN+l)*128) : 0x80000000u; c.v16=(l<16)? (unsigned)((gwv*16+l)*128) : 0x80000000u;
    c.srd1=make_srd(p.w1,S*F*2); c.soff1=(4+MT0)*64*7168; c.srd2=make_srd(p.w2,F*S*2); c.soff2=0; }
  #pragma unroll
  for(int ks=0;ks<2;ks++) c.aX[ks]=lds0+XOFF+(64*wr+i16)*128+(((4*ks+g)^((i16>>1)&7))*16);
  { const int sw2=((q>>1)&1)|((g&1)<<1);
    #pragma unroll
    for(int e=0;e<2;e++) c.aW[e]=lds0+WOFF+g*3072+q*128+(((2*wh+e)^sw2)*32)+8*pp; }
  #pragma unroll
  for(int s=0;s<3;s++)
  #pragma unroll
  for(int e=0;e<2;e++)
  #pragma unroll
  for(int m=0;m<4;m++){ acc[s][e][m]=(f32x4){0,0,0,0}; pin(acc[s][e][m]); }
  STAMP(1);
  c.soffX=0; c.soffW=0;
  if(w<4){ i32x4 srdP=make_srd(p.wphi,32*32*32*2); set_m0<0>(rfl(lds0+WPHI_OFF+w*1024)); asm volatile("s_nop 4"); dma<0>((unsigned)(ct*4096+w*1024+l*16), srdP, 0u); }
  auto tileX=[&](auto tc){ constexpr int T=decltype(tc)::value;
    set_m0<T*XSLOT>(c.mX); nop1(); dma<0>(c.vX[0],c.srdX,c.soffX); dma<1024>(c.vX[1],c.srdX,c.soffX); dma<2048>(c.vX[2],c.srdX,c.soffX); dma<3072>(c.vX[3],c.srdX,c.soffX);
    saddi<128>(c.soffX); };
  auto tileW=[&](auto tc){ constexpr int T=decltype(tc)::value;
    set_m0<T*WSLOT>(c.mW); nop1(); dma<0,QNT>(c.vW[0],c.srdW,c.soffW); dma<1024,QNT>(c.vW[1],c.srdW,c.soffW); dma<2048,QNT>(c.vW[2],c.srdW,c.soffW);
    saddi<64*6144>(c.soffW); };
  tileW(IC(0)); tileX(IC(0));
  if constexpr(TW) touch<0>(c.dT,c.vT,c.srdW,c.soffW);
  tileX(IC(1)); tileW(IC(1));
  waitv<7+TW>(); pin(dC); bar();
  {
    ds_b128<0>(fX[0][0],c.aX[0]); ds_b128<2048>(fX[0][1],c.aX[0]); ds_b128<4096>(fX[0][2],c.aX[0]); ds_b128<6144>(fX[0][3],c.aX[0]);
    auto rd=[&](auto sc,auto ec){ constexpr int s=decltype(sc)::value,e=decltype(ec)::value; u32x2 lo,hi; ds_tr<s*1024>(lo,c.aW[e]); ds_tr<s*1024+512>(hi,c.aW[e]); fW[0][s][e]=cat(lo,hi); };
    rd(IC(0),IC(0)); rd(IC(0),IC(1)); rd(IC(1),IC(0)); rd(IC(1),IC(1)); rd(IC(2),IC(0)); rd(IC(2),IC(1));
    waitl<0>(); }
  tileW(IC(2)); ev_w1(c);
  STAMP(2); RSTAMP(26);
  for(int t=0;t<12;t+=6){
    iter<0,4,true,true,1>(c,acc,fX,fW); iter<1,4+TW,true,true,1>(c,acc,fX,fW); iter<2,4,true,true,1>(c,acc,fX,fW);
    iter<3,4+TW,true,true,1>(c,acc,fX,fW); iter<4,4,true,true,1>(c,acc,fX,fW); iter<5,4+TW,true,true,1>(c,acc,fX,fW); }
  iter<0,4,true,true,EVALL?6:2>(c,acc,fX,fW);
  iter<1,4+EVALL+TW,true,false,2+2>(c,acc,fX,fW);
  iter<2,1,false,false,2+1>(c,acc,fX,fW);
  iter<3,-1,false,false,2+3>(c,acc,fX,fW);
  ev_16<4>(c);
  c.srd2=make_srd(p.wo,D*D*2); ev_16<0>(c);
  asm volatile("s_nop 7\n\ts_nop 7" ::: "memory");
  #pragma unroll
  for(int s=0;s<3;s++)
  #pragma unroll
  for(int e=0;e<2;e++)
  #pragma unroll
  for(int m=0;m<4;m++) pin(acc[s][e][m]);
  STAMP(3); RSTAMP(27);
  bar();
  unsigned epoch;
  epilogue(p,smem,acc,rt,ct,w,c.dT,epoch,stamps);
  return epoch;
}
}

#define MLA 3
#define MNT0 0
#define HPOL 2
namespace mix1 {
constexpr int HSLOT=64*256, WSLOT=64*448;
constexpr int HOFF=0, WOFF=3*HSLOT;
constexpr int LDS_BYTES=WOFF+4*WSLOT;
struct Ctx { unsigned bH[4], bW[2][2], bW6[2], vH[2], vW[4]; i32x4 srdH, srdW; unsigned mH, mW, soffH, soffW; };
template<int POL> DI void dma_w3(Ctx& c){
  if constexpr(POL==1) asm volatile("s_mov_b32 exec_hi, 0\n\tbuffer_load_dwordx4 %0, %1, %2 offen offset:3072 nt lds\n\ts_mov_b32 exec_hi, -1" :: "v"(c.vW[3]), "s"(c.srdW), "s"(c.soffW) : "memory");
  else asm volatile("s_mov_b32 exec_hi, 0\n\tbuffer_load_dwordx4 %0, %1, %2 offen offset:3072 lds\n\ts_mov_b32 exec_hi, -1" :: "v"(c.vW[3]), "s"(c.srdW), "s"(c.soffW) : "memory"); }
template<int SET,int RH,int RW,bool READ,bool DOH,int DH,bool DOW,int DW,int VM>
DI void iter(Ctx& c, f32x4 (&acc)[7][4], u32x4 (&fH)[2][4], u32x4 (&fW)[2][7]){
  constexpr int NX=SET^1;
  if constexpr(VM>=0){ waitv<VM>(); bar(); }
  u32x2 hl[4],hh[4],wl[7],wh[7];
  auto RHf=[&](auto ic,auto hc){ constexpr int i=decltype(ic)::value,h=decltype(hc)::value;
    if constexpr(READ){ if constexpr(h==0) ds_tr<RH*HSLOT>(hl[i],c.bH[i]); else { ds_tr<RH*HSLOT+1024>(hh[i],c.bH[i]); fH[NX][i]=cat(hl[i],hh[i]); } } };
  auto RWf=[&](auto ic,auto hc){ constexpr int i=decltype(ic)::value,h=decltype(hc)::value;
    constexpr int off=(RW&1)*WSLOT+((i<6)?(i&~1)*32:0);
    if constexpr(READ){ const unsigned a=(i<6)? c.bW[RW>>1][i&1] : c.bW6[RW>>1];
      if constexpr(h==0) ds_tr<off>(wl[i],a); else { ds_tr<off+4*448>(wh[i],a); fW[NX][i]=cat(wl[i],wh[i]); } } };
  auto MH=[&](){ if constexpr(DOH) set_m0<DH*HSLOT>(c.mH); };
  auto MW=[&](){ if constexpr(DOW) set_m0<DW*WSLOT>(c.mW); };
  auto DHf=[&](auto kc){ constexpr int k=decltype(kc)::value; if constexpr(DOH) dma<k*1024,HPOL>(c.vH[k],c.srdH,c.soffH); };
  auto DWf=[&](auto kc){ constexpr int k=decltype(kc)::value; if constexpr(DOW){ if constexpr(k<3) dma<k*1024>(c.vW[k],c.srdW,c.soffW); else dma_w3<0>(c); } };
  auto MM=[&](auto ac,auto bc){ constexpr int a=decltype(ac)::value,b=decltype(bc)::value; mfma(acc[a][b],fW[SET][a],fH[SET][b]); };
  MM(IC(0),IC(0)); RHf(IC(0),IC(0)); MH();
  MM(IC(0),IC(1)); RHf(IC(0),IC(1));
  MM(IC(0),IC(2)); RWf(IC(0),IC(0)); DHf(IC(0));
  MM(IC(0),IC(3)); RWf(IC(0),IC(1));
  MM(IC(1),IC(0)); RHf(IC(1),IC(0));
  MM(IC(1),IC(1)); RHf(IC(1),IC(1)); DHf(IC(1));
  MM(IC(1),IC(2)); RWf(IC(1),IC(0));
  MM(IC(1),IC(3)); RWf(IC(1),IC(1)); MW();
  MM(IC(2),IC(0)); RHf(IC(2),IC(0));
  MM(IC(2),IC(1)); RHf(IC(2),IC(1)); DWf(IC(0));
  MM(IC(2),IC(2)); RWf(IC(2),IC(0));
  MM(IC(2),IC(3)); RWf(IC(2),IC(1));
  MM(IC(3),IC(0)); RHf(IC(3),IC(0));
  MM(IC(3),IC(1)); RHf(IC(3),IC(1)); DWf(IC(1));
  MM(IC(3),IC(2)); RWf(IC(3),IC(0));
  MM(IC(3),IC(3)); RWf(IC(3),IC(1));
  MM(IC(4),IC(0)); RWf(IC(4),IC(0));
  MM(IC(4),IC(1)); RWf(IC(4),IC(1)); DWf(IC(2));
  MM(IC(4),IC(2)); RWf(IC(5),IC(0));
  MM(IC(4),IC(3)); RWf(IC(5),IC(1));
  MM(IC(5),IC(0)); RWf(IC(6),IC(0));
  MM(IC(5),IC(1)); RWf(IC(6),IC(1)); DWf(IC(3));
  if constexpr(DOH) saddi<64*2048>(c.soffH);
  if constexpr(DOW) saddi<64*7168>(c.soffW);
  MM(IC(5),IC(2)); MM(IC(5),IC(3));
  MM(IC(6),IC(0)); MM(IC(6),IC(1)); MM(IC(6),IC(2)); MM(IC(6),IC(3));
  if constexpr(READ) waitl<0>();
}
template<int T,bool DOH,bool DOW,int VM,bool READ=true>
DI void it12(Ctx& c, f32x4 (&acc)[7][4], u32x4 (&fH)[2][4], u32x4 (&fW)[2][7]){
  iter<T&1,(T+1)%3,(T+1)&3,READ,DOH,T%3,DOW,T&3,VM>(c,acc,fH,fW); }

DI f32x2 gelu2(f32x2 x){
  const float k1=-2.0f*0.7978845608028654f*1.4426950408889634f, k2=k1*0.044715f;
  const f32x2 t=x*fma2(x*x,bc2(k2),bc2(k1));
  const f32x2 e={__builtin_amdgcn_exp2f(t[0]),__builtin_amdgcn_exp2f(t[1])};
  const f32x2 d=e+bc2(1.0f);
  return x*(f32x2){__builtin_amdgcn_rcpf(d[0]),__builtin_amdgcn_rcpf(d[1])};
}
template<int KG>
DI void epilogue(const Params& p, LAS char* sm, f32x4 (&acc)[7][4], int w, int dt, int ft){
  const int tid=otid(), l=tid&63, dh=w&1, fh=(w>>1)&1;
  const int g=l>>4, i16=l&15;
  { LAS char* wb=sm+w*16384+l*16;
    #pragma unroll
    for(int a=0;a<6;a++){ *(LAS f32x4*)(wb+(a*2+0)*1024)=acc[a][2*(1-KG)]; *(LAS f32x4*)(wb+(a*2+1)*1024)=acc[a][2*(1-KG)+1]; }
    #pragma unroll
    for(int b=0;b<4;b++) *(LAS f32x4*)(wb+(12+b)*1024)=acc[6][b]; }
  __syncthreads();
  const __amdgpu_buffer_rsrc_t rU=make_rsrc(p.ws+WS_U,D*F*2);
  LAS char* rb=sm+(w^4)*16384+l*16;
  const unsigned ub=(unsigned)((dt*112+ft*7)*8192+(64*dh+32*KG+i16)*64+(g&1)*32+(g>>1)*16);
  auto put=[&](const f32x4& x0,const f32x4& x1,unsigned off){
    const pr2 r0=sw16(cvtpk(gelu2(lo2(x0))),cvtpk(gelu2(lo2(x1)))), r1=sw16(cvtpk(gelu2(hi2(x0))),cvtpk(gelu2(hi2(x1))));
    st_sc1(rU, ub+off, (u32x4){r0[0],r1[0],r0[1],r1[1]}); };
  { const int bo=(12+2*KG+fh)*1024+l*16;
    const f32x4 t6=*(LAS f32x4*)(sm+(w&~6)*16384+bo)+*(LAS f32x4*)(sm+((w&~6)|4)*16384+bo);
    const f32x4 t7=*(LAS f32x4*)(sm+((w&~6)|2)*16384+bo)+*(LAS f32x4*)(sm+((w&~6)|6)*16384+bo);
    put(t6,t7,(unsigned)(3*8192+fh*1024)); }
  #pragma unroll
  for(int pr=0;pr<3;pr++)
  #pragma unroll
  for(int b=0;b<2;b++)
    put(acc[2*pr][2*KG+b]+*(LAS f32x4*)(rb+(4*pr+b)*1024), acc[2*pr+1][2*KG+b]+*(LAS f32x4*)(rb+(4*pr+2+b)*1024), (unsigned)((fh*4+pr)*8192+b*1024));
}

DI float dpp_sum(float x){
  auto add=[&](auto cc,auto rm){ x+=__int_as_float(__builtin_amdgcn_update_dpp(0,__float_as_int(x),decltype(cc)::value,decltype(rm)::value,0xf,true)); };
  add(IC(0x111),IC(0xf)); add(IC(0x112),IC(0xf)); add(IC(0x114),IC(0xf)); add(IC(0x118),IC(0xf));
  add(IC(0x142),IC(0xa)); add(IC(0x143),IC(0xc));
  return __int_as_float(__builtin_amdgcn_readlane(__float_as_int(x),63)); }
DI float dot2(unsigned a,unsigned b,float c){ return __builtin_amdgcn_fdot2_f32_bf16(__builtin_bit_cast(bf16x2_t,a),__builtin_bit_cast(bf16x2_t,b),c,false); }

DI unsigned run(const Params& p, char* smem, int dt, int ft, int mi, unsigned& dC, unsigned long long* stamps){
  const unsigned lds0=(unsigned)(size_t)(LAS char*)smem;
  const int tid=threadIdx.x, w=rfl(tid>>6), l=tid&63;
  const int g=l>>4,q=(l&15)>>2,pp=l&3;
  const int dh=w&1, fh=(w>>1)&1, kg=w>>2;
  const int d0=dt*128, f0=ft*224;
  unsigned* flags=(unsigned*)(p.ws+WS_FLG);
  STAMP(9);
  unsigned gen; u32x4 gw0,gw1,xa[2][2]; u32x2 bb[7];
  gld32_sc1(gen, flags);
  { const u16* xr=p.x+(size_t)(mi*16+2*w)*D+l*8;
    gld128<0>(xa[0][0],xr); gld128<1024>(xa[0][1],xr); gld128<2048>(xa[1][0],xr); gld128<3072>(xa[1][1],xr);
    gld128<0>(gw0,p.gw+l*8); gld128<1024>(gw1,p.gw+l*8);
    const u16* bp=p.b1+f0+4*g+fh*128;
    gld64<0>(bb[0],bp); gld64<32>(bb[1],bp); gld64<64>(bb[2],bp); gld64<96>(bb[3],bp); gld64<128>(bb[4],bp); gld64<160>(bb[5],bp);
    gld64<0>(bb[6],p.b1+f0+4*g+(6+fh)*16); }
  Ctx c; f32x4 acc[7][4]; u32x4 fH[2][4], fW[2][7];
  c.srdH=make_srd(p.ws+WS_H,S*D*2); c.srdW=make_srd(p.w1,S*F*2);
  c.mH=rfl(lds0+HOFF+w*2048); c.mW=rfl(lds0+WOFF+w*3584);
  #pragma unroll
  for(int k=0;k<2;k++){ const int row=8*w+4*k+(l>>4); const int sw=(row&3)|(((row>>3)&1)<<2); const int pos=l&15; const int ch=(((pos>>1)^sw)<<1)|(pos&1);
    c.vH[k]=row*2048+d0*2+ch*16-1024*k; }
  #pragma unroll
  for(int k=0;k<4;k++){ const int n=224*w+64*k+l; const int row=n/28,pos=n-row*28; const int ch=pos^(((row>>3)&1)<<1); c.vW[k]=(unsigned)(row*7168+f0*2+ch*16-1024*k); }
  c.soffH=0; c.soffW=0;
  { const int row=32*kg+8*g+q; const int sw=q|((g&1)<<2);
    #pragma unroll
    for(int e=0;e<4;e++) c.bH[e]=lds0+HOFF+row*256+(((4*dh+e)^sw)*32)+8*pp;
    const int b3=g&1;
    #pragma unroll
    for(int hi=0;hi<2;hi++){
      #pragma unroll
      for(int e=0;e<2;e++) c.bW[hi][e]=lds0+WOFF+hi*2*WSLOT+row*448+(((fh*8+e)^b3)*32)+8*pp;
      c.bW6[hi]=lds0+WOFF+hi*2*WSLOT+row*448+(((6+fh)^b3)*32)+8*pp; } }
  #pragma unroll
  for(int k=0;k<4;k++){ pin(c.vW[k]); pin(c.bH[k]); }
  pin(c.vH[0]); pin(c.vH[1]); pin(c.bW[0][0]); pin(c.bW[0][1]); pin(c.bW[1][0]); pin(c.bW[1][1]); pin(c.bW6[0]); pin(c.bW6[1]);
  auto tileW=[&](auto tc){ constexpr int T=decltype(tc)::value;
    set_m0<T*WSLOT>(c.mW); nop1(); dma<0,MNT0>(c.vW[0],c.srdW,c.soffW); dma<1024,MNT0>(c.vW[1],c.srdW,c.soffW); dma<2048,MNT0>(c.vW[2],c.srdW,c.soffW); dma_w3<MNT0>(c);
    saddi<64*7168>(c.soffW); };
  auto tileH=[&](auto tc){ constexpr int T=decltype(tc)::value;
    set_m0<T*HSLOT>(c.mH); nop1(); dma<0,2>(c.vH[0],c.srdH,c.soffH); dma<1024,2>(c.vH[1],c.srdH,c.soffH);
    saddi<64*2048>(c.soffH); };
  unsigned dT=0;
  waitv<7>();
  pin(dC); pin(gen); pin(gw0); pin(gw1); pin(xa[0][0]); pin(xa[0][1]); pin(xa[1][0]); pin(xa[1][1]);
  STAMP(10); RSTAMP(28);
  { const __amdgpu_buffer_rsrc_t rH=make_rsrc(p.ws+WS_H,S*D*2), rG=make_rsrc(p.ws+WS_G,S*4);
    float sum[2], z[2];
    #pragma unroll
    for(int r=0;r<2;r++){ float s=0.f, zz=0.f;
      #pragma unroll
      for(int i=0;i<4;i++){ s=dot2(xa[r][0][i],0x3F803F80u,s); zz=dot2(xa[r][0][i],gw0[i],zz); s=dot2(xa[r][1][i],0x3F803F80u,s); zz=dot2(xa[r][1][i],gw1[i],zz); }
      sum[r]=s; z[r]=zz; }
    #pragma unroll
    for(int r=0;r<2;r++){ sum[r]=dpp_sum(sum[r]); z[r]=dpp_sum(z[r]); }
    f32x2 v[2][8]; float qq[2];
    #pragma unroll
    for(int r=0;r<2;r++){ const f32x2 mean=bc2(sum[r]*(1.0f/D)); f32x2 q2={0.f,0.f};
      #pragma unroll
      for(int i=0;i<4;i++){ v[r][i]=bf2(xa[r][0][i])-mean; v[r][4+i]=bf2(xa[r][1][i])-mean; q2=fma2(v[r][i],v[r][i],q2); q2=fma2(v[r][4+i],v[r][4+i],q2); }
      qq[r]=q2[0]+q2[1]; }
    #pragma unroll
    for(int r=0;r<2;r++){
      const int s=mi*16+2*w+r;
      const f32x2 rs=bc2(__builtin_amdgcn_rsqf(dpp_sum(qq[r])*(1.0f/D)+1e-5f));
      u32x4 o0,o1;
      #pragma unroll
      for(int i=0;i<4;i++){ o0[i]=cvtpk(v[r][i]*rs); o1[i]=cvtpk(v[r][4+i]*rs); }
      st_sc1(rH,(unsigned)(s*2048+l*16),o0); st_sc1(rH,(unsigned)(s*2048+1024+l*16),o1);
      if(l==0) __builtin_amdgcn_raw_buffer_store_b32(__float_as_uint(__builtin_amdgcn_rcpf(1.0f+__builtin_amdgcn_exp2f(-1.4426950408889634f*z[r]))), rG, (unsigned)(s*4), 0, 16);
    } }
  tileW(IC(0));
  STAMP(13);
  waitv<6+4>();
  #pragma unroll
  for(int a=0;a<7;a++){ pin(bb[a]);
    const f32x4 b4= kg? (f32x4){0,0,0,0} : (f32x4){bflo(bb[a][0]),bfhi(bb[a][0]),bflo(bb[a][1]),bfhi(bb[a][1])};
    #pragma unroll
    for(int b=0;b<4;b++){ acc[a][b]=b4; pin(acc[a][b]); } }
  waitv<4>();
  STAMP(14);
  const unsigned epoch=rfl(gen)+1u;
  __syncthreads();
  if(tid==0) flag_st(flags+(FLG_LN+mi)*FST, epoch);
  STAMP(11); RSTAMP(31);
  if(w==0){
    const unsigned* f0p=flags+(size_t)(FLG_LN+l)*FST; const unsigned* f1p=f0p+64*FST;
    for(unsigned spins=0; spins<(1u<<22); ++spins){ const unsigned a=flag_ld(f0p), b=flag_ld(f1p); if(__all(a==epoch && b==epoch)) break; __builtin_amdgcn_s_sleep(1); } }
  bar();
  STAMP(12); RSTAMP(19);
  tileH(IC(0)); tileW(IC(1)); tileH(IC(1));
  waitv<6>();
  bar();
  { auto rd2=[&](auto o0,auto o1,u32x4& f,unsigned a){ u32x2 lo,hi; ds_tr<decltype(o0)::value>(lo,a); ds_tr<decltype(o1)::value>(hi,a); f=cat(lo,hi); };
    rd2(IC(0),IC(1024),fH[0][0],c.bH[0]); rd2(IC(0),IC(1024),fH[0][1],c.bH[1]); rd2(IC(0),IC(1024),fH[0][2],c.bH[2]); rd2(IC(0),IC(1024),fH[0][3],c.bH[3]);
    rd2(IC(0),IC(4*448),fW[0][0],c.bW[0][0]); rd2(IC(0),IC(4*448),fW[0][1],c.bW[0][1]);
    rd2(IC(64),IC(64+4*448),fW[0][2],c.bW[0][0]); rd2(IC(64),IC(64+4*448),fW[0][3],c.bW[0][1]);
    rd2(IC(128),IC(128+4*448),fW[0][4],c.bW[0][0]); rd2(IC(128),IC(128+4*448),fW[0][5],c.bW[0][1]);
    rd2(IC(0),IC(4*448),fW[0][6],c.bW6[0]);
    waitl<0>(); }
  tileW(IC(2)); tileH(IC(2)); tileW(IC(3));
  STAMP(2); RSTAMP(26);
  for(int t=0;t<24;t+=12){
    it12<0,true,true,10>(c,acc,fH,fW); it12<1,true,true,10>(c,acc,fH,fW); it12<2,true,true,10>(c,acc,fH,fW); it12<3,true,true,10>(c,acc,fH,fW);
    it12<4,true,true,10>(c,acc,fH,fW); it12<5,true,true,10>(c,acc,fH,fW); it12<6,true,true,10>(c,acc,fH,fW); it12<7,true,true,10>(c,acc,fH,fW);
    it12<8,true,true,10>(c,acc,fH,fW); it12<9,true,true,10>(c,acc,fH,fW); it12<10,true,true,10>(c,acc,fH,fW); it12<11,true,true,10>(c,acc,fH,fW); }
  it12<0,true,true,10>(c,acc,fH,fW); it12<1,true,true,10>(c,acc,fH,fW); it12<2,true,true,10>(c,acc,fH,fW);
  it12<3,true,true,10>(c,acc,fH,fW);
  it12<4,true,false,10>(c,acc,fH,fW);
  it12<5,false,false,6>(c,acc,fH,fW);
  it12<6,false,false,0>(c,acc,fH,fW);
  it12<7,false,false,-1,false>(c,acc,fH,fW);
  asm volatile("s_nop 7\n\ts_nop 7" ::: "memory");
  #pragma unroll
  for(int a=0;a<7;a++)
  #pragma unroll
  for(int b=0;b<4;b++) pin(acc[a][b]);
  STAMP(3); RSTAMP(27);
  { const i32x4 srd2=make_srd(p.w2,F*S*2); const int lo_=otid()&63; const unsigned v16=(lo_<16)? (unsigned)(((mi*8+w)*16+lo_)*128) : 0x80000000u;
    asm volatile("s_nop 4");
    touch<0>(dT,v16,srd2,5u*16384*128); touch<0>(dT,v16,srd2,6u*16384*128); }
  bar();
  if(kg==0) epilogue<0>(p,(LAS char*)smem,acc,w,dt,ft); else epilogue<1>(p,(LAS char*)smem,acc,w,dt,ft);
  STAMP(4);
  asm volatile("s_waitcnt vmcnt(7)" : "+v"(dT) :: "memory");
  return epoch;
}
}

namespace fin {
constexpr int PART=16384, SLOT=2*PART, NS=4, GB_OFF=NS*SLOT, LDS_BYTES=GB_OFF+2048;
#define OUTPOL 16
#define NB0X 18
constexpr int NB0=NB0X;
struct Src { i32x4 srd; unsigned v[2]; unsigned soff; };
struct Bases { unsigned aR[2], aT[2][4]; };
template<int SET,int SL,bool READ,bool RPA,int DMAK  ,int VM>
DI void iter(const Bases& RB, unsigned mS, unsigned mD, Src& sS, Src& sD, f32x4 (&acc)[4][4], u32x4 (&fS)[2][4], u32x4 (&fD)[2][4]){
  constexpr int NX=SET^1, RS=(SL+1)&3, DS=SL;
  constexpr int RHI=RS>>1, RO=(RS&1)*SLOT;
  if constexpr(VM>=0){ waitv<VM>(); bar(); }
  u32x2 tl[4],th[4];
  auto RR=[&](auto ic){ constexpr int i=decltype(ic)::value;
    if constexpr(READ){ if constexpr(RPA) ds_b128<RO+i*2048>(fS[NX][i],RB.aR[RHI]); else ds_b128<RO+PART+i*2048>(fD[NX][i],RB.aR[RHI]); } };
  auto RT=[&](auto ic,auto hc){ constexpr int i=decltype(ic)::value,h=decltype(hc)::value;
    if constexpr(READ){
      if constexpr(RPA){ if constexpr(h==0) ds_tr<RO+PART>(tl[i],RB.aT[RHI][i]); else { ds_tr<RO+PART+1024>(th[i],RB.aT[RHI][i]); fD[NX][i]=cat(tl[i],th[i]); } }
      else { if constexpr(h==0) ds_tr<RO>(tl[i],RB.aT[RHI][i]); else { ds_tr<RO+1024>(th[i],RB.aT[RHI][i]); fS[NX][i]=cat(tl[i],th[i]); } } } };
  auto MS=[&](){ if constexpr(DMAK!=0) set_m0<DS*SLOT>(mS); };
  auto MD=[&](){ if constexpr(DMAK!=0) set_m0<DS*SLOT>(mD); };
  auto DSs=[&](auto kc){ constexpr int k=decltype(kc)::value; if constexpr(DMAK!=0) dma<k*1024,(DMAK==2)?0:2>(sS.v[k],sS.srd,sS.soff); };
  auto DDd=[&](auto kc){ constexpr int k=decltype(kc)::value; if constexpr(DMAK!=0) dma<k*1024,(DMAK==1)?0:2>(sD.v[k],sD.srd,sD.soff); };
  auto MM=[&](auto ac,auto bc){ constexpr int a=decltype(ac)::value,b=decltype(bc)::value; mfma(acc[a][b],fD[SET][a],fS[SET][b]); };
  auto M1=[&](){ if constexpr(DMAK==1) MS(); else MD(); };
  auto M2=[&](){ if constexpr(DMAK==1) MD(); else MS(); };
  auto D1=[&](auto kc){ if constexpr(DMAK==1) DSs(kc); else DDd(kc); };
  auto D2=[&](auto kc){ if constexpr(DMAK==1) DDd(kc); else DSs(kc); };
  MM(IC(0),IC(0)); RR(IC(0)); M1();
  MM(IC(0),IC(1)); RT(IC(0),IC(0)); D1(IC(0));
  MM(IC(0),IC(2)); RT(IC(0),IC(1));
  MM(IC(0),IC(3)); RR(IC(1)); D1(IC(1));
  MM(IC(1),IC(0)); RT(IC(1),IC(0)); M2();
  MM(IC(1),IC(1)); RT(IC(1),IC(1)); D2(IC(0));
  MM(IC(1),IC(2)); RR(IC(2));
  MM(IC(1),IC(3)); RT(IC(2),IC(0)); D2(IC(1));
  MM(IC(2),IC(0)); RT(IC(2),IC(1));
  MM(IC(2),IC(1)); RR(IC(3));
  if constexpr(DMAK==1){ saddi<262144>(sS.soff); saddi<64*2048>(sD.soff); }
  if constexpr(DMAK==2){ saddi<64*4096>(sS.soff); saddi<16384>(sD.soff); }
  MM(IC(2),IC(2)); RT(IC(3),IC(0));
  MM(IC(2),IC(3)); RT(IC(3),IC(1));
  MM(IC(3),IC(0)); MM(IC(3),IC(1)); MM(IC(3),IC(2)); MM(IC(3),IC(3));
  if constexpr(READ) waitl<0>();
}
template<bool PA> DI void mk_bases(Bases& B, unsigned lds0, int wsd, int wd, int kg){
  const int l=otid()&63, g=l>>4,q=(l&15)>>2,pp=l&3,i16=l&15;
  #pragma unroll
  for(int hi=0;hi<2;hi++){
    const unsigned base=lds0+hi*2*SLOT;
    B.aR[hi]=base+(64*(PA?wsd:wd)+i16)*128+(((4*kg+g)^((i16>>1)&7))*16);
    const int row=32*kg+8*g+q; const int sw=q|((g&1)<<2);
    #pragma unroll
    for(int e=0;e<4;e++) B.aT[hi][e]=base+row*256+(((4*(PA?wd:wsd)+e)^sw)*32)+8*pp;
  }
}
template<bool PA> DI void mk_src(const Params& p, Src& sS, Src& sD, int w, int s0, int dt, int half){
  const int l=otid()&63;
  #pragma unroll
  for(int k=0;k<2;k++){
    const int rowR=16*w+8*k+(l>>3); const int chR=(l&7)^((rowR>>1)&7);
    const int rowT=8*w+4*k+(l>>4); const int sw=(rowT&3)|(((rowT>>3)&1)<<2); const int pos=l&15; const int chT=(((pos>>1)^sw)<<1)|(pos&1);
    if constexpr(PA){ sS.v[k]=(chR>>2)*131072+(s0+rowR)*64+(chR&3)*16-1024*k; sD.v[k]=rowT*2048+dt*256+chT*16-1024*k; }
    else            { sS.v[k]=rowT*4096+s0*2+chT*16-1024*k; sD.v[k]=dt*112*8192+(chR>>2)*8192+rowR*64+(chR&3)*16-1024*k; }
  }
  if constexpr(PA){ sS.srd=make_srd(p.ws+WS_O,S*D*2); sD.srd=make_srd(p.wo,D*D*2); sS.soff=0; sD.soff=0; }
  else { sS.srd=make_srd(p.w2,F*S*2); sD.srd=make_srd(p.ws+WS_U,D*F*2); sS.soff=rfl(half? NB0*64*4096 : 0); sD.soff=rfl(half? NB0*16384 : 0); }
}
template<int HALF>
DI void run(const Params& p, char* smem, int st, int dt, unsigned epoch, int myflag  , unsigned long long* stamps){
  const unsigned lds0=(unsigned)(size_t)(LAS char*)smem;
  LAS char* sm=(LAS char*)smem;
  const int w=rfl(threadIdx.x>>6);
  const int wsd=w&1, wd=(w>>1)&1, kg=w>>2;
  const int s0=st*128, d0=dt*128;
  const unsigned mS=rfl(lds0+w*2048), mD=rfl(lds0+PART+w*2048);
  unsigned* flags=(unsigned*)(p.ws+WS_FLG);
  f32x4 accA[4][4], accB[4][4]; u32x4 fS[2][4], fD[2][4];
  #pragma unroll
  for(int a=0;a<4;a++)
  #pragma unroll
  for(int b=0;b<4;b++){ accB[a][b]=(f32x4){0,0,0,0}; pin(accB[a][b]); if constexpr(HALF==0){ accA[a][b]=(f32x4){0,0,0,0}; pin(accA[a][b]); } }
  STAMP(20);
  auto load_gb=[&](){
    if(w==0){ const int l=otid()&63; i32x4 sg=make_srd(p.ws+WS_G,S*4); set_m0<0>(rfl(lds0+GB_OFF)); asm volatile("s_nop 4"); dma<0,2>((unsigned)(s0*4+l*16),sg,0u); }
    if(w==1){ const int l=otid()&63; i32x4 sb=make_srd(p.b2,S*2); set_m0<0>(rfl(lds0+GB_OFF+1024)); asm volatile("s_nop 4"); dma<0>((unsigned)(s0*2+l*16),sb,0u); } };
  auto tileS=[&](auto slc, auto pac, Src& sS){ constexpr int SLT=decltype(slc)::value; constexpr bool PA=decltype(pac)::value!=0;
    set_m0<SLT*SLOT>(mS); nop1(); dma<0,PA?2:0>(sS.v[0],sS.srd,sS.soff); dma<1024,PA?2:0>(sS.v[1],sS.srd,sS.soff);
    if constexpr(PA) saddi<262144>(sS.soff); else saddi<64*4096>(sS.soff); };
  auto tileD=[&](auto slc, auto pac, Src& sD){ constexpr int SLT=decltype(slc)::value; constexpr bool PA=decltype(pac)::value!=0;
    set_m0<SLT*SLOT>(mD); nop1(); dma<0,PA?0:2>(sD.v[0],sD.srd,sD.soff); dma<1024,PA?0:2>(sD.v[1],sD.srd,sD.soff);
    if constexpr(PA) saddi<64*2048>(sD.soff); else saddi<16384>(sD.soff); };
  auto first_read=[&](auto pac, const Bases& B){ constexpr bool PA=decltype(pac)::value!=0;
    u32x2 l0,h0,l1,h1,l2,h2,l3,h3;
    if constexpr(PA){
      ds_b128<0>(fS[0][0],B.aR[0]); ds_b128<2048>(fS[0][1],B.aR[0]); ds_b128<4096>(fS[0][2],B.aR[0]); ds_b128<6144>(fS[0][3],B.aR[0]);
      ds_tr<PART>(l0,B.aT[0][0]); ds_tr<PART+1024>(h0,B.aT[0][0]); ds_tr<PART>(l1,B.aT[0][1]); ds_tr<PART+1024>(h1,B.aT[0][1]);
      ds_tr<PART>(l2,B.aT[0][2]); ds_tr<PART+1024>(h2,B.aT[0][2]); ds_tr<PART>(l3,B.aT[0][3]); ds_tr<PART+1024>(h3,B.aT[0][3]);
      fD[0][0]=cat(l0,h0); fD[0][1]=cat(l1,h1); fD[0][2]=cat(l2,h2); fD[0][3]=cat(l3,h3);
    } else {
      ds_b128<PART>(fD[0][0],B.aR[0]); ds_b128<PART+2048>(fD[0][1],B.aR[0]); ds_b128<PART+4096>(fD[0][2],B.aR[0]); ds_b128<PART+6144>(fD[0][3],B.aR[0]);
      ds_tr<0>(l0,B.aT[0][0]); ds_tr<1024>(h0,B.aT[0][0]); ds_tr<0>(l1,B.aT[0][1]); ds_tr<1024>(h1,B.aT[0][1]);
      ds_tr<0>(l2,B.aT[0][2]); ds_tr<1024>(h2,B.aT[0][2]); ds_tr<0>(l3,B.aT[0][3]); ds_tr<1024>(h3,B.aT[0][3]);
      fS[0][0]=cat(l0,h0); fS[0][1]=cat(l1,h1); fS[0][2]=cat(l2,h2); fS[0][3]=cat(l3,h3);
    }
    waitl<0>(); };
  #define ITB(P,RD,DK,VM) iter<(P)&1,(P)&3,RD,false,DK,VM>(Bb,mS,mD,bS,bD,accB,fS,fD)
  #define ITX(P,RPA,DK,VM,RB,AC) iter<(P)&1,(P)&3,true,RPA,DK,VM>(RB,mS,mD,aS,aD,AC,fS,fD)
  constexpr int K0=HALF? NB0 : 0, NT=HALF? 56-NB0 : NB0;
  constexpr int PLO=(K0*64)/224;
  constexpr int NL=((NT-4)/4)*4, R=(NT-4)&3;
  Bases Bb; Src bS,bD;
  mk_src<false>(p,bS,bD,w,s0,dt,HALF); mk_bases<false>(Bb,lds0,wsd,wd,kg);
  waitv<0>(); bar();
  if(threadIdx.x==0) flag_st(flags+myflag, epoch);
  STAMP(18);
  tileS(IC(0),IC(0),bS); tileS(IC(1),IC(0),bS); tileS(IC(2),IC(0),bS); tileS(IC(3),IC(0),bS);
  const unsigned* uline=flags+PFLG_U+dt*32;
  auto upto=[&](int kt){ int ph=(kt*64+63)/224; ph=ph>15?15:ph; return ((2u<<ph)-1u)&~((1u<<PLO)-1u); };
  poll_line(uline, upto(K0+7), epoch, otid()&63);
  STAMP(21);
  load_gb();
  tileD(IC(0),IC(0),bD); tileD(IC(1),IC(0),bD); tileD(IC(2),IC(0),bD); tileD(IC(3),IC(0),bD);
  waitv<2>(); bar(); first_read(IC(0),Bb);
  STAMP(22); RSTAMP(29);
  unsigned fv; const unsigned* fl=uline+(otid()&15)*2;
  for(int t=0;t<NL;t+=4){
    gld32_sc1(fv, fl);
    ITB(0,true,2,9); ITB(1,true,2,9); ITB(2,true,2,9); ITB(3,true,2,8);
    pin(fv);
    const unsigned need=upto(K0+t+11< K0+NT? K0+t+11 : K0+NT-1);
    if(((unsigned)__ballot(fv==epoch)&need)!=need) poll_line(uline, need, epoch, otid()&63);
  }
  if constexpr(R==2){ ITB(0,true,2,8); ITB(1,true,2,8); }
  unsigned xf=0;
  if constexpr(HALF==0){
    Bases Ba; Src aS,aD;
    mk_src<true>(p,aS,aD,w,s0,dt,0);
    poll_line(flags+PFLG_O+(st>>1)*32, 0xffffu, epoch, otid()&63);
    ITX(R,false,1,8,Bb,accB); ITX(R+1,false,1,8,Bb,accB); ITX(R+2,false,1,8,Bb,accB);
    mk_bases<true>(Ba,lds0,wsd,wd,kg);
    ITX(R+3,true,1,8,Ba,accB);
    for(int t=0;t<12;t+=4){ ITX(R,true,1,8,Ba,accA); ITX(R+1,true,1,8,Ba,accA); ITX(R+2,true,1,8,Ba,accA); ITX(R+3,true,1,8,Ba,accA); }
    ITX(R,true,0,8,Ba,accA); ITX(R+1,true,0,4,Ba,accA); ITX(R+2,true,0,0,Ba,accA);
    iter<(R+3)&1,(R+3)&3,false,true,0,-1>(Ba,mS,mD,aS,aD,accA,fS,fD);
  } else {
    ITB(R,true,0,8); ITB(R+1,true,0,4); ITB(R+2,true,0,0);
    gld32_sc1(xf, flags+(FLG_X+st*8+dt)*FST);
    ITB(R+3,false,0,-1);
  }
  asm volatile("s_nop 7\n\ts_nop 7" ::: "memory");
  #pragma unroll
  for(int a=0;a<4;a++)
  #pragma unroll
  for(int b=0;b<4;b++){ pin(accB[a][b]); if constexpr(HALF==0) pin(accA[a][b]); }
  STAMP(23); RSTAMP(30);
  const int tid=otid(), l=tid&63, g=l>>4, i16=l&15;
  const __amdgpu_buffer_rsrc_t rX=make_rsrc(p.ws+WS_XCH,128*32768);
  const unsigned xoff=(unsigned)((st*8+dt)*32768+w*4096+l*16);
  u32x4 t[4];
  if constexpr(HALF==1){
    asm volatile("s_waitcnt vmcnt(0)" : "+v"(xf) :: "memory");
    if(!__all(xf==epoch)) poll_flags(flags, FLG_X+st*8+dt, 1, epoch, l);
    #pragma unroll
    for(int b=0;b<4;b++) t[b]=ld_sc1(rX,xoff+b*1024);
  }
  float gt[4], bz[4];
  #pragma unroll
  for(int b=0;b<4;b++){ const int sl=64*wsd+16*b+i16; gt[b]=*(LAS float*)(sm+GB_OFF+sl*4); bz[b]=__uint_as_float(((unsigned)*(LAS u16*)(sm+GB_OFF+1024+sl*2))<<16); }
  #pragma unroll
  for(int a=0;a<4;a++)
  #pragma unroll
  for(int b=0;b<4;b++){ if constexpr(HALF==0) accB[a][b]=accA[a][b]*gt[b]+accB[a][b]*(1.0f-gt[b]); else accB[a][b]=accB[a][b]*(1.0f-gt[b]); }
  bar();
  { LAS char* wb=sm+w*8192+l*16;
    if(kg==0){
      #pragma unroll
      for(int b=0;b<4;b++){ *(LAS f32x4*)(wb+(b*2+0)*1024)=accB[2][b]; *(LAS f32x4*)(wb+(b*2+1)*1024)=accB[3][b]; }
    } else {
      #pragma unroll
      for(int b=0;b<4;b++){ *(LAS f32x4*)(wb+(b*2+0)*1024)=accB[0][b]; *(LAS f32x4*)(wb+(b*2+1)*1024)=accB[1][b]; }
    } }
  __syncthreads();
  f32x4 r[2][4];
  { LAS char* rb=sm+(w^4)*8192+l*16;
    if(kg==0){
      #pragma unroll
      for(int b=0;b<4;b++){ r[0][b]=accB[0][b]+*(LAS f32x4*)(rb+(b*2+0)*1024); r[1][b]=accB[1][b]+*(LAS f32x4*)(rb+(b*2+1)*1024); }
    } else {
      #pragma unroll
      for(int b=0;b<4;b++){ r[0][b]=accB[2][b]+*(LAS f32x4*)(rb+(b*2+0)*1024); r[1][b]=accB[3][b]+*(LAS f32x4*)(rb+(b*2+1)*1024); }
    } }
  STAMP(24);
  if constexpr(HALF==0){
    #pragma unroll
    for(int b=0;b<4;b++) st_sc1(rX,xoff+b*1024,(u32x4){cvtpk(lo2(r[0][b])),cvtpk(hi2(r[0][b])),cvtpk(lo2(r[1][b])),cvtpk(hi2(r[1][b]))});
    asm volatile("s_waitcnt vmcnt(0)" ::: "memory");
    __syncthreads();
    if(tid==0) flag_st(flags+(FLG_X+st*8+dt)*FST, epoch);
  } else {
    const int offg=(g&1)*16+(g>>1)*8;
    #pragma unroll
    for(int b=0;b<4;b++){
      const float add=(1.0f-gt[b])*bz[b];
      const f32x4 x0=r[0][b]+(f32x4){bflo(t[b][0]),bfhi(t[b][0]),bflo(t[b][1]),bfhi(t[b][1])}+add, x1=r[1][b]+(f32x4){bflo(t[b][2]),bfhi(t[b][2]),bflo(t[b][3]),bfhi(t[b][3])}+add;
      const pr2 r0=sw16(cvtpk(lo2(x0)),cvtpk(lo2(x1))), r1=sw16(cvtpk(hi2(x0)),cvtpk(hi2(x1)));
      const u32x4 ov={r0[0],r1[0],r0[1],r1[1]};
      const int s=s0+64*wsd+16*b+i16;
      __builtin_amdgcn_raw_buffer_store_b128(ov, make_rsrc(p.out,S*D*2), (unsigned)((s*D+d0+64*wd+32*kg+offg)*2), 0, OUTPOL);
    }
  }
  STAMP(25);
}
}

#define MAP 4
__global__ __launch_bounds__(512,2) void k_fused(Params p){
  extern __shared__ __attribute__((aligned(16))) char smem[];
  unsigned long long* stamps=(unsigned long long*)(p.ws+WS_STM);
  (void)stamps;
  RSTAMP(0); STAMP(17);
  unsigned dC=0;
  { unsigned lo,hi,sz;
    asm volatile("s_getpc_b64 s[98:99]\n.Lpc0_%=:\n\ts_and_b32 %0, s98, 0xffffff80\n\ts_mov_b32 %1, s99\n\ts_mov_b32 %2, k_code_end-.Lpc0_%=" : "=s"(lo), "=s"(hi), "=s"(sz) :: "s98", "s99", "scc");
    i32x4 sc; sc[0]=(int)lo; sc[1]=(int)(hi&0xffff); sc[2]=(int)sz; sc[3]=0x00020000;
    const unsigned ln=threadIdx.x&63, wv=threadIdx.x>>6;
    const unsigned vo=(ln<2)? (((blockIdx.x>>3)*8+wv)*2+ln)*128u : 0x80000000u;
    touch<0>(dC,vo,sc,0u); }
  const int b=blockIdx.x, xcd=b&7, j=b>>3;
  const int jj=j>>1;
  const bool isq=(j&1)==0; const int pc=2*xcd+(jj>>3), pr=jj&7, mi=xcd*16+jj;
  unsigned epoch; int myflag;
  if(isq){ epoch=qkv::run(p,smem,pr,pc,dC,stamps); myflag=PFLG_O+(pr*16+pc)*2; } else { epoch=mix1::run(p,smem,pr,pc,mi,dC,stamps); myflag=PFLG_U+(pr*16+pc)*2; }
  RSTAMP(16);
  const int st=((xcd&3)&1)*8+(j&7), dtf=((xcd&3)>>1)*4+(j>>3);
  const bool h0=xcd<4;
  if(h0) fin::run<0>(p,smem,st,dtf,epoch,myflag,stamps); else fin::run<1>(p,smem,st,dtf,epoch,myflag,stamps);
  if(b==0 && threadIdx.x==0) flag_st((unsigned*)(p.ws+WS_FLG), epoch);
  RSTAMP(15);
}

extern "C" __device__ __attribute__((noinline,used)) void k_code_end(){ }

extern "C" void kernel_launch(void* const* d_in, const int* in_sizes, int n_in, void* d_out, int out_size, void* d_ws, size_t ws_size, hipStream_t stream){
  (void)in_sizes; (void)n_in; (void)out_size; (void)ws_size;
  Params p;
  p.x=(const u16*)d_in[0]; p.wqkv=(const u16*)d_in[1]; p.wphi=(const u16*)d_in[2]; p.wo=(const u16*)d_in[3];
  p.w1=(const u16*)d_in[5]; p.b1=(const u16*)d_in[6]; p.w2=(const u16*)d_in[7]; p.b2=(const u16*)d_in[8]; p.gw=(const u16*)d_in[9];
  p.out=(u16*)d_out; p.ws=(char*)d_ws;
  constexpr int L1_=(qkv::LDS_BYTES>mix1::LDS_BYTES)?qkv::LDS_BYTES:mix1::LDS_BYTES;
  constexpr int LDS_ALL=(L1_>fin::LDS_BYTES)?L1_:fin::LDS_BYTES;
  (void)hipFuncSetAttribute((const void*)k_fused, hipFuncAttributeMaxDynamicSharedMemorySize, LDS_ALL);
  k_fused<<<256,512,LDS_ALL,stream>>>(p);
}
```

```cpp
#include <hip/hip_runtime.h>
#include <hip/hip_bf16.h>
#include <type_traits>
#include <utility>

#define LAS __attribute__((address_space(3)))
#define DI __device__ __forceinline__
#define IC(n) std::integral_constant<int,(n)>{}
typedef unsigned short u16;
typedef float f32x4 __attribute__((ext_vector_type(4)));
typedef float f32x2 __attribute__((ext_vector_type(2)));
typedef int i32x4 __attribute__((ext_vector_type(4)));
typedef unsigned u32x4 __attribute__((ext_vector_type(4)));
typedef unsigned u32x2 __attribute__((ext_vector_type(2)));
typedef short bf16x8 __attribute__((ext_vector_type(8)));
typedef __bf16 bf16x2_t __attribute__((ext_vector_type(2)));

constexpr int S=2048, D=1024, F=3584;
constexpr size_t WS_H   = 0;
constexpr size_t WS_U   = WS_H + (size_t)S*D*2;
constexpr size_t WS_O   = WS_U + (size_t)D*F*2;
constexpr size_t WS_KVP = WS_O + (size_t)S*D*2;
constexpr size_t WS_ZP  = WS_KVP + (size_t)16*8*8*1024;
constexpr size_t WS_G   = WS_ZP + (size_t)16*8*2*64*4;
constexpr size_t WS_XCH = WS_G + 8192;
constexpr size_t WS_FLG = WS_XCH + (size_t)128*65536;
constexpr int FST = 32;
constexpr int FLG_LN = 64;
constexpr int FLG_KV = 192;
constexpr int FLG_X  = 320;
constexpr int PFLG_O = 16384;
constexpr int PFLG_U = 16384+512;

template<int OFF> DI void ds_tr(u32x2& d, unsigned a){ asm volatile("ds_read_b64_tr_b16 %0, %1 offset:%2" : "=v"(d) : "v"(a), "i"(OFF)); }
template<int OFF> DI void ds_b128(u32x4& d, unsigned a){ asm volatile("ds_read_b128 %0, %1 offset:%2" : "=v"(d) : "v"(a), "i"(OFF)); }
template<int OFF> DI void ds_w128(unsigned a, const f32x4& v){ asm volatile("ds_write_b128 %0, %1 offset:%2" :: "v"(a), "v"(v), "i"(OFF) : "memory"); }
DI void mfma(f32x4& c, const u32x4& a, const u32x4& b){ asm volatile("v_mfma_f32_16x16x32_bf16 %0, %1, %2, %0" : "+v"(c) : "v"(a), "v"(b)); }
DI f32x4 mfma_b(const u32x4& a, const u32x4& b, const f32x4& c){ return __builtin_amdgcn_mfma_f32_16x16x32_bf16(__builtin_bit_cast(bf16x8,a), __builtin_bit_cast(bf16x8,b), c, 0,0,0); }
template<int IMM,int POL=0> DI void dma(unsigned voff, const i32x4& srd, unsigned soff){
  if constexpr(POL==2) asm volatile("buffer_load_dwordx4 %0, %1, %2 offen offset:%3 sc1 lds" :: "v"(voff), "s"(srd), "s"(soff), "i"(IMM) : "memory");
  else asm volatile("buffer_load_dwordx4 %0, %1, %2 offen offset:%3 lds" :: "v"(voff), "s"(srd), "s"(soff), "i"(IMM) : "memory"); }
template<int IMM> DI void touch(unsigned& d, unsigned voff, const i32x4& srd, unsigned soff){
  asm volatile("buffer_load_dword %0, %1, %2, %3 offen offset:%4" : "+v"(d) : "v"(voff), "s"(srd), "s"(soff), "i"(IMM) : "memory"); }
DI void touch2(unsigned& d, unsigned v1, const i32x4& r1, unsigned s1, unsigned v2, const i32x4& r2, unsigned s2){
  asm volatile("buffer_load_dword %0, %1, %2, %3 offen\n\tbuffer_load_dword %0, %4, %5, %6 offen" : "+v"(d) : "v"(v1), "s"(r1), "s"(s1), "v"(v2), "s"(r2), "s"(s2) : "memory"); }
template<int V> DI void set_m0(unsigned base){ asm volatile("s_mov_b32 m0, %0" :: "s"(base+V)); }
DI void nop1(){ asm volatile("s_nop 0"); }
template<int N> DI void waitv(){ asm volatile("s_waitcnt vmcnt(%0)" :: "n"(N) : "memory"); }
template<int N> DI void waitl(){ asm volatile("s_waitcnt lgkmcnt(%0)" :: "n"(N) : "memory"); }
DI void bar(){ asm volatile("s_barrier" ::: "memory"); }
DI void lbar(){ asm volatile("s_waitcnt lgkmcnt(0)\n\ts_barrier" ::: "memory"); }
template<int V> DI void saddi(unsigned& x){ asm volatile("s_add_u32 %0, %0, %1" : "+s"(x) : "i"(V) : "scc"); }
template<int A,int B> DI void saddi2(unsigned& x,unsigned& y){ asm volatile("s_add_u32 %0, %0, %2\n\ts_add_u32 %1, %1, %3" : "+s"(x), "+s"(y) : "i"(A), "i"(B) : "scc"); }
DI void waitl0(){ __builtin_amdgcn_s_waitcnt(0xC07F); }
DI void pin(f32x4& x){ asm volatile("" : "+v"(x)); }
DI void pin(u32x4& x){ asm volatile("" : "+v"(x)); }
DI void pin(u32x2& x){ asm volatile("" : "+v"(x)); }
DI void pin(unsigned& x){ asm volatile("" : "+v"(x)); }
DI u32x4 cat(u32x2 lo,u32x2 hi){ return __builtin_shufflevector(lo,hi,0,1,2,3); }
DI unsigned cvtpk(float lo,float hi){ f32x2 v={lo,hi}; bf16x2_t b=__builtin_convertvector(v,bf16x2_t); return __builtin_bit_cast(unsigned,b); }
DI unsigned cvtpk(f32x2 v){ bf16x2_t b=__builtin_convertvector(v,bf16x2_t); return __builtin_bit_cast(unsigned,b); }
DI float bflo(unsigned x){ return __uint_as_float(x<<16); }
DI float bfhi(unsigned x){ return __uint_as_float(x&0xffff0000u); }
DI f32x2 bf2(unsigned x){ return (f32x2){bflo(x),bfhi(x)}; }
DI f32x2 lo2(const f32x4& v){ return __builtin_shufflevector(v,v,0,1); }
DI f32x2 hi2(const f32x4& v){ return __builtin_shufflevector(v,v,2,3); }
DI f32x2 fma2(f32x2 a,f32x2 b,f32x2 c){ return __builtin_elementwise_fma(a,b,c); }
DI f32x2 bc2(float x){ return (f32x2){x,x}; }
DI int otid(){ int t=threadIdx.x; asm volatile("" : "+v"(t)); return t; }
DI unsigned rfl(unsigned x){ return __builtin_amdgcn_readfirstlane(x); }
DI i32x4 make_srd(const void* p, unsigned bytes){
  unsigned long long a=(unsigned long long)p; i32x4 s;
  s[0]=(int)rfl((unsigned)a); s[1]=(int)(rfl((unsigned)(a>>32))&0xffff); s[2]=(int)bytes; s[3]=0x00020000; return s; }
DI __amdgpu_buffer_rsrc_t make_rsrc(const void* p, unsigned bytes){ return __builtin_amdgcn_make_buffer_rsrc((void*)p, 0, bytes, 0x00020000); }
DI void st_sc1(__amdgpu_buffer_rsrc_t r, unsigned off, const u32x4& v){ __builtin_amdgcn_raw_buffer_store_b128(v, r, off, 0, 16); }
DI u32x4 ld_sc1(__amdgpu_buffer_rsrc_t r, unsigned off){ return __builtin_amdgcn_raw_buffer_load_b128(r, off, 0, 16); }
template<int OFF> DI void gld128(u32x4& d, const void* p){ asm volatile("global_load_dwordx4 %0, %1, off offset:%2" : "=v"(d) : "v"(p), "i"(OFF) : "memory"); }
template<int OFF> DI void gld64(u32x2& d, const void* p){ asm volatile("global_load_dwordx2 %0, %1, off offset:%2" : "=v"(d) : "v"(p), "i"(OFF) : "memory"); }
DI void gld32_sc1(unsigned& d, const void* p){ asm volatile("global_load_dword %0, %1, off sc1" : "=v"(d) : "v"(p) : "memory"); }
template<int IMM> DI void bld32_sc1(unsigned& d, unsigned voff, const i32x4& srd, unsigned soff){
  asm volatile("buffer_load_dword %0, %1, %2, %3 offen offset:%4 sc1" : "=v"(d) : "v"(voff), "s"(srd), "s"(soff), "i"(IMM) : "memory"); }
typedef unsigned pr2 __attribute__((ext_vector_type(2)));
DI pr2 sw16(unsigned a,unsigned b){ auto r=__builtin_amdgcn_permlane16_swap(a,b,false,false); return (pr2){r[0],r[1]}; }
DI pr2 sw32(unsigned a,unsigned b){ auto r=__builtin_amdgcn_permlane32_swap(a,b,false,false); return (pr2){r[0],r[1]}; }
DI float mx2(float a,float b){ return __builtin_elementwise_maximum(a,b); }
DI float mx8(const f32x4& a,const f32x4& b){ return mx2(mx2(mx2(mx2(a[0],a[1]),a[2]),mx2(mx2(a[3],b[0]),b[1])),mx2(b[2],b[3])); }
DI float mn2(float a,float b){ return __builtin_elementwise_minimum(a,b); }
DI float mn8(const f32x4& a,const f32x4& b){ return mn2(mn2(mn2(mn2(a[0],a[1]),a[2]),mn2(mn2(a[3],b[0]),b[1])),mn2(b[2],b[3])); }
DI unsigned flag_ld(const unsigned* p){ return __hip_atomic_load(p, __ATOMIC_RELAXED, __HIP_MEMORY_SCOPE_AGENT); }
DI void flag_st(unsigned* p, unsigned v){ __hip_atomic_store(p, v, __ATOMIC_RELAXED, __HIP_MEMORY_SCOPE_AGENT); }
DI void poll_flags(const unsigned* flags, int first, int n, unsigned epoch, int lane){
  const unsigned* p = flags + (size_t)(first + (lane<n?lane:0))*FST;
  for(unsigned spins=0; spins<(1u<<22); ++spins){
    unsigned v=flag_ld(p);
    if(__all(v==epoch)) break;
    __builtin_amdgcn_s_sleep(1);
  }
}
DI void poll_line(const unsigned* line, unsigned mask, unsigned epoch, int lane){
  const unsigned* p = line + (lane&15)*2;
  for(unsigned spins=0; spins<(1u<<22); ++spins){
    const unsigned v=flag_ld(p);
    if(((unsigned)__ballot(v==epoch)&mask)==mask) break;
    __builtin_amdgcn_s_sleep(1);
  }
}

constexpr int NB0X=18;
constexpr int C3PF=16;
constexpr int QLA=2;
constexpr int XSP=2, WG0=1, WSP=9, MG0=1, MSP=5, FG0=1, FSP=4;

struct Params {
  const u16* x; const u16* wqkv; const u16* wphi; const u16* wo; const u16* w1; const u16* b1; const u16* w2; const u16* b2; const u16* gw;
  u16* out; char* ws;
};

DI void c3_touch(const Params& p, int w, unsigned& d){
  const int xcd=blockIdx.x&7;
  constexpr int LN=((NB0X>C3PF? NB0X : C3PF)*64*4+255)/256;
  const int l=otid()&63, n=((int)(blockIdx.x>>3)*8+w)*LN+l, row=n>>2;
  const int half=xcd>>2;
  const int K0=half? NB0X : 0, rows=(half? C3PF : NB0X)*64;
  const unsigned vo=(l<LN && row<rows)? (unsigned)((K0*64+row)*4096+((xcd&1)*8+1+4*(n&1))*256+((n>>1)&1)*128) : 0x80000000u;
  const i32x4 s2=make_srd(p.w2,F*S*2);
  asm volatile("s_nop 4"); touch<0>(d,vo,s2,0u);
}

constexpr int EVN=7;
namespace qkv {
constexpr int XSLOT=256*128, WSLOT=64*384;
constexpr int XOFF=0, WOFF=2*XSLOT;
constexpr int WPHI_OFF=WOFF+3*WSLOT;
constexpr int LDS_BYTES=WPHI_OFF+8192+2048;
struct Ctx { unsigned aX[2], aW[2], vX[4], vW[3], vT, v7, v16, dT; i32x4 srdX, srdW, srd1, srd2; unsigned mX, mW, soffX, soffW, soff1, soff2;
};
DI void ev_w1(Ctx& c){ touch<0>(c.dT,c.v7,c.srd1,c.soff1); saddi<EVN*1024*128>(c.soff1); }
template<int E> DI void ev_16(Ctx& c){ c.soff2=E*16384*128; touch<0>(c.dT,c.v16,c.srd2,c.soff2); }

template<int SET,int RX,int RW,int RKS,bool READ,bool DOX,int DXS,bool DOW,int DWS,bool TCH,int EV>
DI void step(Ctx& c, f32x4 (&acc)[3][2][4], u32x4 (&fX)[2][4], u32x4 (&fW)[2][3][2]){
  constexpr int NX=SET^1;
  u32x2 wl[3][2], wh[3][2];
  auto RXf=[&](auto mc){ constexpr int m=decltype(mc)::value; if constexpr(READ) ds_b128<RX*XSLOT+m*2048>(fX[NX][m], c.aX[RKS]); };
  auto RWf=[&](auto sc,auto ec,auto tc){ constexpr int s=decltype(sc)::value,e=decltype(ec)::value,t=decltype(tc)::value;
    if constexpr(READ){ constexpr int o=RW*WSLOT+RKS*12288+s*1024;
      if constexpr(t==0) ds_tr<o>(wl[s][e], c.aW[e]); else { ds_tr<o+512>(wh[s][e], c.aW[e]); fW[NX][s][e]=cat(wl[s][e],wh[s][e]); } } };
  auto MX=[&](){ if constexpr(DOX) set_m0<DXS*XSLOT>(c.mX); };
  auto MW=[&](){ if constexpr(DOW) set_m0<DWS*WSLOT>(c.mW); };
  auto DX=[&](auto kc){ constexpr int k=decltype(kc)::value; if constexpr(DOX) dma<k*1024>(c.vX[k], c.srdX, c.soffX); };
  auto DW=[&](auto kc){ constexpr int k=decltype(kc)::value; if constexpr(DOW) dma<k*1024>(c.vW[k], c.srdW, c.soffW); };
  auto MM=[&](auto sc,auto ec,auto mc){ constexpr int s=decltype(sc)::value,e=decltype(ec)::value,m=(e&1)? 3-decltype(mc)::value : decltype(mc)::value;
    if constexpr(s<2) mfma(acc[s][e][m], fW[SET][s][e], fX[SET][m]); else mfma(acc[s][e][m], fX[SET][m], fW[SET][s][e]); };
  constexpr int S_=XSP;
  auto G=[&](auto gc){ constexpr int g=decltype(gc)::value;
    if constexpr(DOX){ if constexpr(g==0) MX(); if constexpr(g==1) DX(IC(0)); if constexpr(g==1+S_) DX(IC(1)); if constexpr(g==1+2*S_) DX(IC(2)); if constexpr(g==1+3*S_) DX(IC(3)); }
    if constexpr(DOW){ if constexpr(g==WG0-1) MW(); if constexpr(g==WG0) DW(IC(0)); if constexpr(g==WG0+WSP) DW(IC(1)); if constexpr(g==WG0+2*WSP) DW(IC(2)); } };
  MM(IC(0),IC(0),IC(0)); RXf(IC(0)); G(IC(0));
  MM(IC(0),IC(0),IC(1)); RWf(IC(0),IC(0),IC(0)); G(IC(1));
  MM(IC(0),IC(0),IC(2)); RWf(IC(0),IC(0),IC(1)); G(IC(2));
  MM(IC(0),IC(0),IC(3)); RXf(IC(1)); G(IC(3));
  MM(IC(0),IC(1),IC(0)); RWf(IC(0),IC(1),IC(0)); G(IC(4));
  MM(IC(0),IC(1),IC(1)); RWf(IC(0),IC(1),IC(1)); G(IC(5));
  MM(IC(0),IC(1),IC(2)); RXf(IC(2)); G(IC(6));
  MM(IC(0),IC(1),IC(3)); RXf(IC(3)); G(IC(7));
  MM(IC(1),IC(0),IC(0)); RWf(IC(1),IC(0),IC(0)); G(IC(8));
  MM(IC(1),IC(0),IC(1)); RWf(IC(1),IC(0),IC(1)); G(IC(9));
  MM(IC(1),IC(0),IC(2)); RWf(IC(1),IC(1),IC(0)); G(IC(10));
  MM(IC(1),IC(0),IC(3)); RWf(IC(1),IC(1),IC(1)); G(IC(11));
  MM(IC(1),IC(1),IC(0)); RWf(IC(2),IC(0),IC(0)); G(IC(12));
  MM(IC(1),IC(1),IC(1)); RWf(IC(2),IC(0),IC(1)); G(IC(13));
  MM(IC(1),IC(1),IC(2)); RWf(IC(2),IC(1),IC(0)); G(IC(14));
  MM(IC(1),IC(1),IC(3)); RWf(IC(2),IC(1),IC(1)); G(IC(15));
  MM(IC(2),IC(0),IC(0)); G(IC(16));
  MM(IC(2),IC(0),IC(1)); G(IC(17));
  MM(IC(2),IC(0),IC(2)); G(IC(18));
  constexpr bool E1=(EV==1);
  if constexpr(TCH && E1) touch2(c.dT, c.vT, c.srdW, c.soffW, c.v7, c.srd1, c.soff1);
  else if constexpr(TCH) touch<0>(c.dT, c.vT, c.srdW, c.soffW);
  else if constexpr(E1) touch<0>(c.dT, c.v7, c.srd1, c.soff1);
  MM(IC(2),IC(0),IC(3)); G(IC(19));
  MM(IC(2),IC(1),IC(0));
  if constexpr(EV>=2) ev_16<EV-2>(c);
  MM(IC(2),IC(1),IC(1));
  if constexpr(DOX && E1) saddi2<128,EVN*1024*128>(c.soffX,c.soff1);
  else if constexpr(DOX) saddi<128>(c.soffX);
  if constexpr(DOW) saddi<64*6144>(c.soffW);
  MM(IC(2),IC(1),IC(2)); MM(IC(2),IC(1),IC(3));
  waitl0();
}
template<int T6,int VM,bool DOX,bool DOW,int EV>
DI void iter(Ctx& c, f32x4 (&acc)[3][2][4], u32x4 (&fX)[2][4], u32x4 (&fW)[2][3][2]){
  step<0,T6&1,T6%3,1,true,false,0,DOX,(T6+2)%3,false,0>(c,acc,fX,fW);
  if constexpr(VM>=0){ waitv<VM>(); bar(); }
  step<1,(T6+1)&1,(T6+1)%3,0,(VM>=0),DOX,T6&1,false,0,DOW&&((T6&1)==0),EV>(c,acc,fX,fW);
}

template<int N,int M0>
DI void feat(const u32x4 (&Ap)[2], const f32x4 (&a)[2][4], f32x2 (&ep)[N][4], f32x2 (&en)[N][4]){
  constexpr float L2E=1.4426950408889634f;
  const f32x4 zero={0,0,0,0};
  f32x4 P0[N], P1[N]; float mx[N], nm[N];
  #pragma unroll
  for(int i=0;i<N;i++){ const int m=M0+i;
    u32x4 B; B[0]=cvtpk(lo2(a[0][m])); B[1]=cvtpk(hi2(a[0][m])); B[2]=cvtpk(lo2(a[1][m])); B[3]=cvtpk(hi2(a[1][m]));
    P0[i]=mfma_b(Ap[0],B,zero); P1[i]=mfma_b(Ap[1],B,zero); }
  #pragma unroll
  for(int i=0;i<N;i++){ mx[i]=mx8(P0[i],P1[i]); nm[i]=-mn8(P0[i],P1[i]); }
  pr2 r[N]; float t[N];
  #pragma unroll
  for(int i=0;i<N;i++){ r[i]=sw32(__float_as_uint(mx[i]),__float_as_uint(nm[i])); t[i]=mx2(__uint_as_float(r[i][0]),__uint_as_float(r[i][1])); }
  #pragma unroll
  for(int i=0;i<N;i++){ r[i]=sw16(__float_as_uint(t[i]),__float_as_uint(t[i])); t[i]=mx2(__uint_as_float(r[i][0]),__uint_as_float(r[i][1])); }
  #pragma unroll
  for(int i=0;i<N;i++){ r[i]=sw32(__float_as_uint(t[i]),__float_as_uint(t[i])); }
  #pragma unroll
  for(int i=0;i<N;i++){
    const f32x2 cp=bc2(__uint_as_float(r[i][0])*-L2E), cn=bc2(__uint_as_float(r[i][1])*-L2E), kp=bc2(L2E), kn=bc2(-L2E);
    const f32x2 q[4]={lo2(P0[i]),hi2(P0[i]),lo2(P1[i]),hi2(P1[i])};
    #pragma unroll
    for(int k=0;k<4;k++){
      const f32x2 tp=fma2(q[k],kp,cp), tn=fma2(q[k],kn,cn);
      ep[i][k]=(f32x2){__builtin_amdgcn_exp2f(tp[0]),__builtin_amdgcn_exp2f(tp[1])};
      en[i][k]=(f32x2){__builtin_amdgcn_exp2f(tn[0]),__builtin_amdgcn_exp2f(tn[1])};
    } }
}
DI u32x4 pack8(const f32x2 (&v)[4]){ return (u32x4){cvtpk(v[0]),cvtpk(v[1]),cvtpk(v[2]),cvtpk(v[3])}; }
DI float dotk(unsigned x,unsigned k,float c){ return __builtin_amdgcn_fdot2_f32_bf16(__builtin_bit_cast(bf16x2_t,x),__builtin_bit_cast(bf16x2_t,k),c,false); }

template<class LATE>
DI void epilogue(const Params& p, char* smem, f32x4 (&acc)[3][2][4], int rt, int ct, int w, unsigned& dT, unsigned& epoch_out, LATE late_touches){
  const unsigned lds0=(unsigned)(size_t)(LAS char*)smem;
  LAS char* sm=(LAS char*)smem;
  const int tid=otid(), l=tid&63;
  const int g=l>>4,q=(l&15)>>2,pp=l&3,i16=l&15;
  const int wr=w&3, wh=w>>2;
  constexpr int TPHI=0, VREG=65536, SHR=98304;
  char* ws=p.ws;
  unsigned* flags=(unsigned*)(ws+WS_FLG);
  unsigned gen; gld32_sc1(gen, flags);
  u32x4 Ap[2];
  { const unsigned b=lds0+WPHI_OFF+wh*2048+(4*g+q)*64+pp*8;
    u32x2 l0,h0,l1,h1; ds_tr<0>(l0,b); ds_tr<1024>(h0,b); ds_tr<32>(l1,b); ds_tr<32+1024>(h1,b);
    #pragma unroll
    for(int mp=0;mp<2;mp++)
    #pragma unroll
    for(int e=0;e<2;e++){
      u32x4 fv; fv[0]=cvtpk(lo2(acc[2][e][2*mp])); fv[1]=cvtpk(hi2(acc[2][e][2*mp])); fv[2]=cvtpk(lo2(acc[2][e][2*mp+1])); fv[3]=cvtpk(hi2(acc[2][e][2*mp+1]));
      *(LAS u32x4*)(sm+VREG+w*4096+(mp*2+e)*1024+l*16)=fv;
    }
    asm volatile("s_waitcnt lgkmcnt(0)" : "+v"(l0),"+v"(h0),"+v"(l1),"+v"(h1) :: "memory");
    Ap[0]=cat(l0,h0); Ap[1]=cat(l1,h1); }
  const u32x4 ones={0x3F803F80u,0x3F803F80u,0x3F803F80u,0x3F803F80u};
  const f32x4 zero={0,0,0,0};
  late_touches();
  { const int swp=(((i16>>1)&3)<<2)|(((i16>>3)&1)<<1)|(i16&1);
    f32x2 ep[4][4], en[4][4];
    feat<4,0>(Ap,acc[1],ep,en);
    f32x4 sp[4], sn[4];
    #pragma unroll
    for(int m=0;m<4;m++){ sp[m]=mfma_b(ones,pack8(ep[m]),zero); sn[m]=mfma_b(ones,pack8(en[m]),zero); }
    #pragma unroll
    for(int m=0;m<4;m++){
      const f32x2 rp=bc2(__builtin_amdgcn_rcpf(sp[m][0])), rn=bc2(__builtin_amdgcn_rcpf(sn[m][0]));
      LAS char* row=sm+TPHI+w*8192+(16*m+i16)*128;
      *(LAS u32x2*)(row+(((0+g)^swp)*8))=(u32x2){cvtpk(ep[m][0]*rp),cvtpk(ep[m][1]*rp)};
      *(LAS u32x2*)(row+(((4+g)^swp)*8))=(u32x2){cvtpk(ep[m][2]*rp),cvtpk(ep[m][3]*rp)};
      *(LAS u32x2*)(row+(((8+g)^swp)*8))=(u32x2){cvtpk(en[m][0]*rn),cvtpk(en[m][1]*rn)};
      *(LAS u32x2*)(row+(((12+g)^swp)*8))=(u32x2){cvtpk(en[m][2]*rn),cvtpk(en[m][3]*rn)};
    } }
  lbar();
  f32x4 kv0={0,0,0,0},kv1={0,0,0,0},zz={0,0,0,0};
  { const int r15=4*g+q; const int swr=(((r15>>1)&3)<<2)|(((r15>>3)&1)<<1)|(r15&1);
    const unsigned ab=lds0+TPHI+wh*4*8192+r15*128+(((4*wr+pp)^swr)*8);
    const unsigned vb=lds0+VREG+wh*4*4096+l*16;
    u32x2 a[4][4]; u32x4 v[4][4];
    auto rd=[&](auto sc){ constexpr int s_=decltype(sc)::value;
      ds_tr<s_*8192+0>(a[s_][0],ab); ds_tr<s_*8192+2048>(a[s_][1],ab); ds_tr<s_*8192+4096>(a[s_][2],ab); ds_tr<s_*8192+6144>(a[s_][3],ab);
      ds_b128<s_*4096>(v[s_][0],vb); ds_b128<s_*4096+1024>(v[s_][1],vb); ds_b128<s_*4096+2048>(v[s_][2],vb); ds_b128<s_*4096+3072>(v[s_][3],vb); };
    rd(IC(0)); rd(IC(1)); rd(IC(2)); rd(IC(3));
    waitl<0>();
    #pragma unroll
    for(int s_=0;s_<4;s_++){
      #pragma unroll
      for(int k=0;k<4;k++){ pin(a[s_][k]); pin(v[s_][k]); }
      const u32x4 A0=cat(a[s_][0],a[s_][1]), A1=cat(a[s_][2],a[s_][3]);
      kv0=mfma_b(A0,v[s_][0],kv0); kv1=mfma_b(A0,v[s_][1],kv1); zz=mfma_b(A0,ones,zz);
      kv0=mfma_b(A1,v[s_][2],kv0); kv1=mfma_b(A1,v[s_][3],kv1); zz=mfma_b(A1,ones,zz); }
  }
  const __amdgpu_buffer_rsrc_t rKV=make_rsrc(ws+WS_KVP,16*8*8*1024), rZ=make_rsrc(ws+WS_ZP,16*8*2*64*4);
  st_sc1(rKV,(unsigned)(((ct*8+rt)*8+w)*1024+l*16),(u32x4){cvtpk(lo2(kv0)),cvtpk(hi2(kv0)),cvtpk(lo2(kv1)),cvtpk(hi2(kv1))});
  if(i16==0) st_sc1(rZ,(unsigned)((((ct*8+rt)*2+wh)*64+16*wr+4*g)*4),__builtin_bit_cast(u32x4,zz));
  u32x4 phq[4][2]; float rq[4][2];
  auto qblk=[&](auto nc,auto mc){ constexpr int n=decltype(nc)::value, m0=decltype(mc)::value;
    f32x2 ep[n][4], en[n][4];
    feat<n,m0>(Ap,acc[0],ep,en);
    #pragma unroll
    for(int i=0;i<n;i++){ phq[m0+i][0]=pack8(ep[i]); phq[m0+i][1]=pack8(en[i]); }
    #pragma unroll
    for(int i=0;i<n;i++){ const f32x4 sp=mfma_b(ones,phq[m0+i][0],zero), sn=mfma_b(ones,phq[m0+i][1],zero);
      rq[m0+i][0]=__builtin_amdgcn_rcpf(sp[0]); rq[m0+i][1]=__builtin_amdgcn_rcpf(sn[0]); } };
  qblk(IC(2),IC(0));
  asm volatile("" : "+v"(phq[0][0]), "+v"(phq[0][1]), "+v"(phq[1][0]), "+v"(phq[1][1]), "+v"(rq[0][0]), "+v"(rq[0][1]), "+v"(rq[1][0]), "+v"(rq[1][1]));
  asm volatile("s_waitcnt vmcnt(0)" : "+v"(gen), "+v"(dT) :: "memory");
  const unsigned epoch=rfl(gen)+1u; epoch_out=epoch;
  __syncthreads();
  if(tid==0) flag_st(flags+(FLG_KV+ct*8+rt)*FST, epoch);
  qblk(IC(1),IC(2));
  poll_flags(flags, FLG_KV+ct*8, 8, epoch, l);
  { u32x4 t0[8],tz[8];
    #pragma unroll
    for(int r=0;r<8;r++){ t0[r]=ld_sc1(rKV,(unsigned)(((ct*8+r)*8+w)*1024+l*16)); tz[r]=ld_sc1(rZ,(unsigned)((((ct*8+r)*2+wh)*64+16*wr+4*g)*4)); }
    asm volatile("" ::: "memory");
    qblk(IC(1),IC(3));
    float s[8]={0,0,0,0,0,0,0,0}; f32x4 sz={0,0,0,0};
    unsigned klo=0x00003F80u, khi=0x3F800000u; asm volatile("" : "+v"(klo), "+v"(khi));
    #pragma unroll
    for(int r=0;r<8;r++){
      #pragma unroll
      for(int k=0;k<4;k++){ s[2*k]=dotk(t0[r][k],klo,s[2*k]); s[2*k+1]=dotk(t0[r][k],khi,s[2*k+1]); }
      sz+=__builtin_bit_cast(f32x4,tz[r]); }
    LAS char* sb=sm+SHR+w*1536+l*8;
    *(LAS u32x2*)(sb)=(u32x2){cvtpk(s[0],s[1]),cvtpk(s[2],s[3])};
    *(LAS u32x2*)(sb+512)=(u32x2){cvtpk(s[4],s[5]),cvtpk(s[6],s[7])};
    *(LAS u32x2*)(sb+1024)=(u32x2){cvtpk(lo2(sz)),cvtpk(hi2(sz))};
  }
  lbar();
  { u32x4 kf[2][2], zf[2];
    #pragma unroll
    for(int sg=0;sg<2;sg++){
      LAS char* b0=sm+SHR+(wh*4+2*sg)*1536+l*8; LAS char* b1=b0+1536;
      kf[sg][0]=cat(*(LAS u32x2*)(b0),*(LAS u32x2*)(b1)); kf[sg][1]=cat(*(LAS u32x2*)(b0+512),*(LAS u32x2*)(b1+512)); zf[sg]=cat(*(LAS u32x2*)(b0+1024),*(LAS u32x2*)(b1+1024)); }
    const __amdgpu_buffer_rsrc_t rO=make_rsrc(ws+WS_O,S*D*2);
    const int offg=(g&1)*32+(g>>1)*16;
    f32x4 n0p[4],n0n[4],n1p[4],n1n[4],dp[4],dn[4];
    #pragma unroll
    for(int m=0;m<4;m++){
      dp[m]=mfma_b(zf[0],phq[m][0],zero); dn[m]=mfma_b(zf[1],phq[m][1],zero);
      n0p[m]=mfma_b(kf[0][0],phq[m][0],zero); n0n[m]=mfma_b(kf[1][0],phq[m][1],zero);
      n1p[m]=mfma_b(kf[0][1],phq[m][0],zero); n1n[m]=mfma_b(kf[1][1],phq[m][1],zero); }
    #pragma unroll
    for(int m=0;m<4;m++){
      const float rd=__builtin_amdgcn_rcpf(dp[m][0]*rq[m][0]+dn[m][0]*rq[m][1]+1e-6f);
      const f32x2 a=bc2(rq[m][0]*rd), b=bc2(rq[m][1]*rd);
      const unsigned a0=cvtpk(fma2(lo2(n0p[m]),a,lo2(n0n[m])*b)), a1=cvtpk(fma2(hi2(n0p[m]),a,hi2(n0n[m])*b));
      const unsigned b0=cvtpk(fma2(lo2(n1p[m]),a,lo2(n1n[m])*b)), b1=cvtpk(fma2(hi2(n1p[m]),a,hi2(n1n[m])*b));
      const pr2 r0=sw16(a0,b0), r1=sw16(a1,b1);
      const u32x4 ov={r0[0],r1[0],r0[1],r1[1]};
      const int tok=rt*256+64*wr+16*m+i16;
      st_sc1(rO,(unsigned)((ct*2+wh)*131072+tok*64+offg),ov);
    }
  }
}

DI unsigned run(const Params& p, char* smem, int rt, int ct){
  const unsigned lds0=(unsigned)(size_t)(LAS char*)smem;
  const int tid=threadIdx.x, w=rfl(tid>>6), l=tid&63;
  const int wr=w&3, wh=w>>2;
  Ctx c; f32x4 acc[3][2][4]; u32x4 fX[2][4], fW[2][3][2];
  c.srdX=make_srd(p.x,S*D*2); c.srdW=make_srd(p.wqkv,D*3*D*2);
  c.mX=rfl(lds0+XOFF+w*4096); c.mW=rfl(lds0+WOFF+w*3072);
  #pragma unroll
  for(int k=0;k<4;k++){ const int row=32*w+8*k+(l>>3); const int ch=(l&7)^((row>>1)&7); c.vX[k]=(rt*256+row)*2048+ch*16-1024*k; }
  #pragma unroll
  for(int s=0;s<3;s++){ const int row=8*w+(l>>3); const int sw=((row>>1)&1)|(((row>>3)&1)<<1); const int pos=l&7; const int ch=(((pos>>1)^sw)<<1)|(pos&1);
    c.vW[s]=row*6144+s*2048+ct*128+ch*16-1024*s; }
  auto setup=[&](const int l){
  const int g=l>>4,q=(l&15)>>2,pp=l&3,i16=l&15;
  c.vT=(l<6)? (unsigned)(QLA*64*6144+(((ct*8+rt)*8+w)*6+l)*128) : 0x80000000u;
  { const int gwv=(ct*8+rt)*8+w;
    c.v7=(l<EVN)? (unsigned)((gwv*EVN+l)*128) : 0x80000000u;
    c.v16=(l<16)? (unsigned)((gwv*16+l)*128) : 0x80000000u;
    { const int id=((ct-(ct>3)-(ct>11))*8+rt)*8+w;
      auto orow=[](int r){ return r<8? 24+r : 80+r; };
      if(l==7){ const int r=id/56; c.v7=(unsigned)(orow(r)*7168+(id-r*56)*128); }
      if(l>=16 && l<19){ const int q=id*3+(l-16); if(q<2048) c.v16=(unsigned)(orow(q>>7)*16384+(q&127)*128); }
      if(l==6 && id<768){ const int r=id/48; c.vT=(unsigned)(QLA*64*6144+orow(r)*6144+(id-r*48)*128); }
      if((ct&7)==3){ c.v7=0x80000000u; c.v16=0x80000000u; c.vT=0x80000000u; } }
    c.srd1=make_srd(p.w1,S*F*2); c.soff1=0; c.srd2=make_srd(p.w2,F*S*2); c.soff2=0; }
  #pragma unroll
  for(int ks=0;ks<2;ks++) c.aX[ks]=lds0+XOFF+(64*wr+i16)*128+(((4*ks+g)^((i16>>1)&7))*16);
  { const int sw2=((q>>1)&1)|((g&1)<<1);
    #pragma unroll
    for(int e=0;e<2;e++) c.aW[e]=lds0+WOFF+g*3072+q*128+(((2*wh+e)^sw2)*32)+8*pp; }
  #pragma unroll
  for(int s=0;s<3;s++)
  #pragma unroll
  for(int e=0;e<2;e++)
  #pragma unroll
  for(int m=0;m<4;m++){ acc[s][e][m]=(f32x4){0,0,0,0}; pin(acc[s][e][m]); }
  };
  c.dT=0;
  c.soffX=0; c.soffW=0;
  auto tileP=[&](){
    i32x4 srdP=make_srd(p.wphi,32*32*32*2); set_m0<0>(rfl(lds0+WPHI_OFF+w*1024)); asm volatile("s_nop 4");
    dma<0>(w<4? (unsigned)(ct*4096+w*1024+l*16) : 0x80000000u, srdP, 0u); };
  auto tileX=[&](auto tc){ constexpr int T=decltype(tc)::value;
    set_m0<T*XSLOT>(c.mX); nop1(); dma<0>(c.vX[0],c.srdX,c.soffX); dma<1024>(c.vX[1],c.srdX,c.soffX); dma<2048>(c.vX[2],c.srdX,c.soffX); dma<3072>(c.vX[3],c.srdX,c.soffX);
    saddi<128>(c.soffX); };
  auto tileW=[&](auto tc){ constexpr int T=decltype(tc)::value;
    set_m0<T*WSLOT>(c.mW); nop1(); dma<0>(c.vW[0],c.srdW,c.soffW); dma<1024>(c.vW[1],c.srdW,c.soffW); dma<2048>(c.vW[2],c.srdW,c.soffW);
    saddi<64*6144>(c.soffW); };
  tileW(IC(0)); tileX(IC(0));
  setup(otid()&63);
  tileX(IC(1)); tileW(IC(1)); tileP();
  touch<0>(c.dT,c.vT,c.srdW,(unsigned)(64*6144));
  ev_w1(c); ev_w1(c);
  waitv<4+3+1+1+2>(); bar();
  {
    ds_b128<0>(fX[0][0],c.aX[0]); ds_b128<2048>(fX[0][1],c.aX[0]); ds_b128<4096>(fX[0][2],c.aX[0]); ds_b128<6144>(fX[0][3],c.aX[0]);
    auto rd=[&](auto sc,auto ec){ constexpr int s=decltype(sc)::value,e=decltype(ec)::value; u32x2 lo,hi; ds_tr<s*1024>(lo,c.aW[e]); ds_tr<s*1024+512>(hi,c.aW[e]); fW[0][s][e]=cat(lo,hi); };
    rd(IC(0),IC(0)); rd(IC(0),IC(1)); rd(IC(1),IC(0)); rd(IC(1),IC(1)); rd(IC(2),IC(0)); rd(IC(2),IC(1));
    waitl<0>(); }
  ev_w1(c);
  for(int t=0;t<12;t+=6){
    iter<0,4,true,true,1>(c,acc,fX,fW); iter<1,5,true,true,1>(c,acc,fX,fW); iter<2,4,true,true,1>(c,acc,fX,fW);
    iter<3,5,true,true,1>(c,acc,fX,fW); iter<4,4,true,true,1>(c,acc,fX,fW); iter<5,5,true,true,1>(c,acc,fX,fW); }
  iter<0,4,true,true,1>(c,acc,fX,fW);
  iter<1,5,true,false,0>(c,acc,fX,fW);
  iter<2,0,false,false,2+1>(c,acc,fX,fW);
  ev_16<4>(c);
  { const i32x4 so=make_srd(p.wo,D*D*2); asm volatile("s_nop 4"); touch<0>(c.dT,c.v16,so,0u); }
  iter<3,-1,false,false,0>(c,acc,fX,fW);
  asm volatile("s_nop 7\n\ts_nop 7" ::: "memory");
  #pragma unroll
  for(int s=0;s<3;s++)
  #pragma unroll
  for(int e=0;e<2;e++)
  #pragma unroll
  for(int m=0;m<4;m++) pin(acc[s][e][m]);
  bar();
  unsigned epoch;
  epilogue(p,smem,acc,rt,ct,w,c.dT,epoch,[&](){
    c.srd2=make_srd(p.w2,F*S*2); asm volatile("s_nop 4"); ev_16<3>(c);
    if constexpr(C3PF>0) c3_touch(p,w,c.dT); });
  return epoch;
}
}

namespace mix1 {
constexpr int HSLOT=64*256, WSLOT=64*448;
constexpr int HOFF=0, WOFF=3*HSLOT;
constexpr int LDS_BYTES=WOFF+4*WSLOT;
struct Ctx { unsigned bH[4], bW[2][2], bW6[2], vH[2], vW[4]; i32x4 srdH, srdW; unsigned mH, mW, soffH, soffW;
};
DI void dma_w3(Ctx& c){
  asm volatile("s_mov_b32 exec_hi, 0\n\tbuffer_load_dwordx4 %0, %1, %2 offen offset:3072 lds\n\ts_mov_b32 exec_hi, -1" :: "v"(c.vW[3]), "s"(c.srdW), "s"(c.soffW) : "memory"); }
template<int SET,int RH,int RW,bool READ,bool DOH,int DH,bool DOW,int DW,int VM>
DI void iter(Ctx& c, f32x4 (&acc)[7][4], u32x4 (&fH)[2][4], u32x4 (&fW)[2][7]){
  constexpr int NX=SET^1;
  if constexpr(VM>=0){ waitv<VM>(); bar(); }
  u32x2 hl[4],hh[4],wl[7],wh[7];
  auto RHf=[&](auto ic,auto hc){ constexpr int i=decltype(ic)::value,h=decltype(hc)::value;
    if constexpr(READ){ if constexpr(h==0) ds_tr<RH*HSLOT>(hl[i],c.bH[i]); else { ds_tr<RH*HSLOT+1024>(hh[i],c.bH[i]); fH[NX][i]=cat(hl[i],hh[i]); } } };
  auto RWf=[&](auto ic,auto hc){ constexpr int i=decltype(ic)::value,h=decltype(hc)::value;
    constexpr int off=(RW&1)*WSLOT+((i<6)?(i&~1)*32:0);
    if constexpr(READ){ const unsigned a=(i<6)? c.bW[RW>>1][i&1] : c.bW6[RW>>1];
      if constexpr(h==0) ds_tr<off>(wl[i],a); else { ds_tr<off+4*448>(wh[i],a); fW[NX][i]=cat(wl[i],wh[i]); } } };
  auto MH=[&](){ if constexpr(DOH) set_m0<DH*HSLOT>(c.mH); };
  auto MW=[&](){ if constexpr(DOW) set_m0<DW*WSLOT>(c.mW); };
  auto DHf=[&](auto kc){ constexpr int k=decltype(kc)::value; if constexpr(DOH) dma<k*1024,2>(c.vH[k],c.srdH,c.soffH); };
  auto DWf=[&](auto kc){ constexpr int k=decltype(kc)::value; if constexpr(DOW){ if constexpr(k<3) dma<k*1024>(c.vW[k],c.srdW,c.soffW); else dma_w3(c); } };
  auto MM=[&](auto ac,auto bc){ constexpr int a=decltype(ac)::value,b=(a&1)? 3-decltype(bc)::value : decltype(bc)::value; mfma(acc[a][b],fW[SET][a],fH[SET][b]); };
  auto G=[&](auto gc){ constexpr int g=decltype(gc)::value;
    if constexpr(g==MG0-1) MH();
    if constexpr(g==MG0) DHf(IC(0));
    if constexpr(g==MG0+MSP) DHf(IC(1));
    if constexpr(g==MG0+MSP+1) MW();
    if constexpr(g==MG0+2*MSP) DWf(IC(0));
    if constexpr(g==MG0+3*MSP) DWf(IC(1));
    if constexpr(g==MG0+4*MSP) DWf(IC(2));
    if constexpr(g==MG0+5*MSP) DWf(IC(3));
    if constexpr(g==27){ if constexpr(DOH && DOW) saddi2<64*2048,64*7168>(c.soffH,c.soffW); else if constexpr(DOH) saddi<64*2048>(c.soffH); } };
  MM(IC(0),IC(0)); RHf(IC(0),IC(0)); G(IC(0));
  MM(IC(0),IC(1)); RHf(IC(0),IC(1)); G(IC(1));
  MM(IC(0),IC(2)); RWf(IC(0),IC(0)); G(IC(2));
  MM(IC(0),IC(3)); RWf(IC(0),IC(1)); G(IC(3));
  MM(IC(1),IC(0)); RHf(IC(1),IC(0)); G(IC(4));
  MM(IC(1),IC(1)); RHf(IC(1),IC(1)); G(IC(5));
  MM(IC(1),IC(2)); RWf(IC(1),IC(0)); G(IC(6));
  MM(IC(1),IC(3)); RWf(IC(1),IC(1)); G(IC(7));
  MM(IC(2),IC(0)); RHf(IC(2),IC(0)); G(IC(8));
  MM(IC(2),IC(1)); RHf(IC(2),IC(1)); G(IC(9));
  MM(IC(2),IC(2)); RWf(IC(2),IC(0)); G(IC(10));
  MM(IC(2),IC(3)); RWf(IC(2),IC(1)); G(IC(11));
  MM(IC(3),IC(0)); RHf(IC(3),IC(0)); G(IC(12));
  MM(IC(3),IC(1)); RHf(IC(3),IC(1)); G(IC(13));
  MM(IC(3),IC(2)); RWf(IC(3),IC(0)); G(IC(14));
  MM(IC(3),IC(3)); RWf(IC(3),IC(1)); G(IC(15));
  MM(IC(4),IC(0)); RWf(IC(4),IC(0)); G(IC(16));
  MM(IC(4),IC(1)); RWf(IC(4),IC(1)); G(IC(17));
  MM(IC(4),IC(2)); RWf(IC(5),IC(0)); G(IC(18));
  MM(IC(4),IC(3)); RWf(IC(5),IC(1)); G(IC(19));
  MM(IC(5),IC(0)); RWf(IC(6),IC(0)); G(IC(20));
  MM(IC(5),IC(1)); RWf(IC(6),IC(1)); G(IC(21));
  MM(IC(5),IC(2)); G(IC(22)); MM(IC(5),IC(3)); G(IC(23));
  MM(IC(6),IC(0)); G(IC(24)); MM(IC(6),IC(1)); G(IC(25)); MM(IC(6),IC(2)); G(IC(26)); MM(IC(6),IC(3)); G(IC(27));
  waitl0();
}
template<int T,bool DOH,bool DOW,int VM>
DI void it12(Ctx& c, f32x4 (&acc)[7][4], u32x4 (&fH)[2][4], u32x4 (&fW)[2][7]){
  iter<T&1,(T+1)%3,(T+1)&3,true,DOH,T%3,DOW,T&3,VM>(c,acc,fH,fW); }

DI f32x2 gelu2(f32x2 x){
  const float k1=-2.0f*0.7978845608028654f*1.4426950408889634f, k2=k1*0.044715f;
  const f32x2 t=x*fma2(x*x,bc2(k2),bc2(k1));
  const f32x2 e={__builtin_amdgcn_exp2f(t[0]),__builtin_amdgcn_exp2f(t[1])};
  const f32x2 d=e+bc2(1.0f);
  return x*(f32x2){__builtin_amdgcn_rcpf(d[0]),__builtin_amdgcn_rcpf(d[1])};
}
DI void last(f32x4 (&acc)[7][4], u32x4 (&fH)[2][4], u32x4 (&fW)[2][7], unsigned wa, unsigned kgo){
  constexpr int SET=1;
  auto MM=[&](auto ac,auto bc){ constexpr int a=decltype(ac)::value,b=decltype(bc)::value; mfma(acc[a][b],fW[SET][a],fH[SET][b]); };
  const unsigned wk=wa+kgo, wo=wa-kgo;
  auto W6=[&](auto bc){ constexpr int b=decltype(bc)::value; ds_w128<(12+b)*1024>(b<2? wk : wo, acc[6][b]); };
  auto WS=[&](auto ac,auto bc){ constexpr int a=decltype(ac)::value,b=decltype(bc)::value; ds_w128<(a*2+b)*1024>(wa, acc[a][2+b]); };
  bar();
  MM(IC(6),IC(0)); MM(IC(6),IC(1)); MM(IC(6),IC(2)); MM(IC(6),IC(3));
  MM(IC(0),IC(2)); MM(IC(0),IC(3)); MM(IC(1),IC(2)); MM(IC(1),IC(3)); MM(IC(2),IC(2)); MM(IC(2),IC(3));
  MM(IC(3),IC(2)); MM(IC(3),IC(3)); MM(IC(4),IC(2)); MM(IC(4),IC(3)); MM(IC(5),IC(2)); MM(IC(5),IC(3));
  W6(IC(0)); MM(IC(0),IC(0)); W6(IC(1)); MM(IC(0),IC(1)); W6(IC(2)); MM(IC(1),IC(0)); W6(IC(3)); MM(IC(1),IC(1));
  WS(IC(0),IC(0)); MM(IC(2),IC(0)); WS(IC(0),IC(1)); MM(IC(2),IC(1)); WS(IC(1),IC(0)); MM(IC(3),IC(0)); WS(IC(1),IC(1)); MM(IC(3),IC(1));
  WS(IC(2),IC(0)); WS(IC(2),IC(1)); MM(IC(4),IC(0)); WS(IC(3),IC(0)); MM(IC(4),IC(1)); WS(IC(3),IC(1)); MM(IC(5),IC(0));
  WS(IC(4),IC(0)); MM(IC(5),IC(1)); WS(IC(4),IC(1)); WS(IC(5),IC(0)); WS(IC(5),IC(1));
}
DI void epilogue(const Params& p, LAS char* sm, f32x4 (&acc)[7][4], int w, int dt, int ft){
  const int tid=otid(), l=tid&63, dh=w&1, fh=(w>>1)&1, kg=w>>2;
  const int g=l>>4, i16=l&15;
  lbar();
  const __amdgpu_buffer_rsrc_t rU=make_rsrc(p.ws+WS_U,D*F*2);
  LAS char* rb=sm+(w^4)*16384+l*16;
  const unsigned ub=(unsigned)((dt*112+ft*7)*8192+(64*dh+32*kg+i16)*64+(g&1)*32+(g>>1)*16);
  auto put=[&](const f32x4& x0,const f32x4& x1,unsigned off){
    const pr2 r0=sw16(cvtpk(gelu2(lo2(x0))),cvtpk(gelu2(lo2(x1)))), r1=sw16(cvtpk(gelu2(hi2(x0))),cvtpk(gelu2(hi2(x1))));
    st_sc1(rU, ub+off, (u32x4){r0[0],r1[0],r0[1],r1[1]}); };
  { const int bo=(12+2*kg+fh)*1024+l*16;
    const f32x4 t6=*(LAS f32x4*)(sm+(w&~6)*16384+bo)+*(LAS f32x4*)(sm+((w&~6)|4)*16384+bo);
    const f32x4 t7=*(LAS f32x4*)(sm+((w&~6)|2)*16384+bo)+*(LAS f32x4*)(sm+((w&~6)|6)*16384+bo);
    put(t6,t7,(unsigned)(3*8192+fh*1024)); }
  #pragma unroll
  for(int pr=0;pr<3;pr++)
  #pragma unroll
  for(int b=0;b<2;b++)
    put(acc[2*pr][b]+*(LAS f32x4*)(rb+(4*pr+b)*1024), acc[2*pr+1][b]+*(LAS f32x4*)(rb+(4*pr+2+b)*1024), (unsigned)((fh*4+pr)*8192+b*1024));
}

DI float dpp_sum(float x){
  auto add=[&](auto cc,auto rm){ x+=__int_as_float(__builtin_amdgcn_update_dpp(0,__float_as_int(x),decltype(cc)::value,decltype(rm)::value,0xf,true)); };
  add(IC(0x111),IC(0xf)); add(IC(0x112),IC(0xf)); add(IC(0x114),IC(0xf)); add(IC(0x118),IC(0xf));
  add(IC(0x142),IC(0xa)); add(IC(0x143),IC(0xc));
  return __int_as_float(__builtin_amdgcn_readlane(__float_as_int(x),63)); }
DI float dot2(unsigned a,unsigned b,float c){ return __builtin_amdgcn_fdot2_f32_bf16(__builtin_bit_cast(bf16x2_t,a),__builtin_bit_cast(bf16x2_t,b),c,false); }

DI unsigned run(const Params& p, char* smem, int dt, int ft, int mi){
  const unsigned lds0=(unsigned)(size_t)(LAS char*)smem;
  const int tid=threadIdx.x, w=rfl(tid>>6), l=tid&63;
  const int g=l>>4,q=(l&15)>>2,pp=l&3;
  const int dh=w&1, fh=(w>>1)&1, kg=w>>2;
  const int d0=dt*128, f0=ft*224;
  unsigned* flags=(unsigned*)(p.ws+WS_FLG);
  unsigned gen; u32x4 gw0,gw1,xa[2][2]; u32x2 bb[7];
  gld32_sc1(gen, flags);
  { const u16* xr=p.x+(size_t)(mi*16+2*w)*D+l*8;
    gld128<0>(xa[0][0],xr); gld128<1024>(xa[0][1],xr); gld128<2048>(xa[1][0],xr); gld128<3072>(xa[1][1],xr);
    gld128<0>(gw0,p.gw+l*8); gld128<1024>(gw1,p.gw+l*8);
    const u16* bp=p.b1+f0+4*g+fh*128;
    gld64<0>(bb[0],bp); gld64<32>(bb[1],bp); gld64<64>(bb[2],bp); gld64<96>(bb[3],bp); gld64<128>(bb[4],bp); gld64<160>(bb[5],bp);
    gld64<0>(bb[6],p.b1+f0+4*g+(6+fh)*16); }
  Ctx c; f32x4 acc[7][4]; u32x4 fH[2][4], fW[2][7];
  c.srdH=make_srd(p.ws+WS_H,S*D*2); c.srdW=make_srd(p.w1,S*F*2);
  c.mH=rfl(lds0+HOFF+w*2048); c.mW=rfl(lds0+WOFF+w*3584);
  #pragma unroll
  for(int k=0;k<2;k++){ const int row=8*w+4*k+(l>>4); const int sw=(row&3)|(((row>>3)&1)<<2); const int pos=l&15; const int ch=(((pos>>1)^sw)<<1)|(pos&1);
    c.vH[k]=row*2048+d0*2+ch*16-1024*k; }
  #pragma unroll
  for(int k=0;k<4;k++){ const int n=224*w+64*k+l; const int row=n/28,pos=n-row*28; const int ch=pos^(((row>>3)&1)<<1); c.vW[k]=(unsigned)(row*7168+f0*2+ch*16-1024*k); }
  c.soffH=0; c.soffW=0;
  { const int row=32*kg+8*g+q; const int sw=q|((g&1)<<2);
    #pragma unroll
    for(int e=0;e<4;e++) c.bH[e]=lds0+HOFF+row*256+(((4*dh+(e^(2*kg)))^sw)*32)+8*pp;
    const int b3=g&1;
    #pragma unroll
    for(int hi=0;hi<2;hi++){
      #pragma unroll
      for(int e=0;e<2;e++) c.bW[hi][e]=lds0+WOFF+hi*2*WSLOT+row*448+(((fh*8+e)^b3)*32)+8*pp;
      c.bW6[hi]=lds0+WOFF+hi*2*WSLOT+row*448+(((6+fh)^b3)*32)+8*pp; } }
  #pragma unroll
  for(int k=0;k<4;k++){ pin(c.vW[k]); pin(c.bH[k]); }
  pin(c.vH[0]); pin(c.vH[1]); pin(c.bW[0][0]); pin(c.bW[0][1]); pin(c.bW[1][0]); pin(c.bW[1][1]); pin(c.bW6[0]); pin(c.bW6[1]);
  auto tileW=[&](auto tc){ constexpr int T=decltype(tc)::value;
    set_m0<T*WSLOT>(c.mW); nop1(); dma<0>(c.vW[0],c.srdW,c.soffW); dma<1024>(c.vW[1],c.srdW,c.soffW); dma<2048>(c.vW[2],c.srdW,c.soffW); dma_w3(c);
    saddi<64*7168>(c.soffW); };
  auto tileH=[&](auto tc){ constexpr int T=decltype(tc)::value;
    set_m0<T*HSLOT>(c.mH); nop1(); dma<0,2>(c.vH[0],c.srdH,c.soffH); dma<1024,2>(c.vH[1],c.srdH,c.soffH);
    saddi<64*2048>(c.soffH); };
  unsigned dT=0;
  waitv<7>();
  pin(gen); pin(gw0); pin(gw1); pin(xa[0][0]); pin(xa[0][1]); pin(xa[1][0]); pin(xa[1][1]);
  { const __amdgpu_buffer_rsrc_t rH=make_rsrc(p.ws+WS_H,S*D*2), rG=make_rsrc(p.ws+WS_G,S*4);
    float sum[2], z[2];
    #pragma unroll
    for(int r=0;r<2;r++){ float s=0.f, zz=0.f;
      #pragma unroll
      for(int i=0;i<4;i++){ s=dot2(xa[r][0][i],0x3F803F80u,s); zz=dot2(xa[r][0][i],gw0[i],zz); s=dot2(xa[r][1][i],0x3F803F80u,s); zz=dot2(xa[r][1][i],gw1[i],zz); }
      sum[r]=s; z[r]=zz; }
    #pragma unroll
    for(int r=0;r<2;r++){ sum[r]=dpp_sum(sum[r]); z[r]=dpp_sum(z[r]); }
    f32x2 v[2][8]; float qq[2];
    #pragma unroll
    for(int r=0;r<2;r++){ const f32x2 mean=bc2(sum[r]*(1.0f/D)); f32x2 q2={0.f,0.f};
      #pragma unroll
      for(int i=0;i<4;i++){ v[r][i]=bf2(xa[r][0][i])-mean; v[r][4+i]=bf2(xa[r][1][i])-mean; q2=fma2(v[r][i],v[r][i],q2); q2=fma2(v[r][4+i],v[r][4+i],q2); }
      qq[r]=q2[0]+q2[1]; }
    #pragma unroll
    for(int r=0;r<2;r++){
      const int s=mi*16+2*w+r;
      const f32x2 rs=bc2(__builtin_amdgcn_rsqf(dpp_sum(qq[r])*(1.0f/D)+1e-5f));
      u32x4 o0,o1;
      #pragma unroll
      for(int i=0;i<4;i++){ o0[i]=cvtpk(v[r][i]*rs); o1[i]=cvtpk(v[r][4+i]*rs); }
      st_sc1(rH,(unsigned)(s*2048+l*16),o0); st_sc1(rH,(unsigned)(s*2048+1024+l*16),o1);
      if(l==0) __builtin_amdgcn_raw_buffer_store_b32(__float_as_uint(__builtin_amdgcn_rcpf(1.0f+__builtin_amdgcn_exp2f(-1.4426950408889634f*z[r]))), rG, (unsigned)(s*4), 0, 16);
    } }
  const i32x4 srdF=make_srd(flags,131072);
  const unsigned vF=(w==0)? (unsigned)((FLG_LN+l)*FST*4) : 0x80000000u, vE=(w==0)? (unsigned)((FLG_LN+(l&15))*FST*4) : 0x80000000u;
  unsigned pa, pb, pc;
  asm volatile("s_nop 4");
  bld32_sc1<0>(pa,vE,srdF,0u);
  waitv<0>();
  const unsigned epoch=rfl(gen)+1u;
  pin(pa);
  bar();
  if(tid==0) flag_st(flags+(FLG_LN+mi)*FST, epoch);
  tileW(IC(0)); tileW(IC(1)); tileW(IC(2));
  if(w==0 && !__all(pa==epoch)){
    const unsigned* fep=flags+(size_t)(FLG_LN+(l&15))*FST;
    for(unsigned spins=0; spins<(1u<<22); ++spins){ if(__all(flag_ld(fep)==epoch)) break; __builtin_amdgcn_s_sleep(1); } }
  bar();
  tileH(IC(0)); tileH(IC(1));
  #pragma unroll
  for(int a=0;a<7;a++){ pin(bb[a]);
    const f32x4 b4= kg? (f32x4){0,0,0,0} : (f32x4){bflo(bb[a][0]),bfhi(bb[a][0]),bflo(bb[a][1]),bfhi(bb[a][1])};
    #pragma unroll
    for(int b=0;b<4;b++){ acc[a][b]=b4; pin(acc[a][b]); } }
  waitv<2>();
  bar();
  { auto rd2=[&](auto o0,auto o1,u32x4& f,unsigned a){ u32x2 lo,hi; ds_tr<decltype(o0)::value>(lo,a); ds_tr<decltype(o1)::value>(hi,a); f=cat(lo,hi); };
    rd2(IC(0),IC(1024),fH[0][0],c.bH[0]); rd2(IC(0),IC(1024),fH[0][1],c.bH[1]); rd2(IC(0),IC(1024),fH[0][2],c.bH[2]); rd2(IC(0),IC(1024),fH[0][3],c.bH[3]);
    rd2(IC(0),IC(4*448),fW[0][0],c.bW[0][0]); rd2(IC(0),IC(4*448),fW[0][1],c.bW[0][1]);
    rd2(IC(64),IC(64+4*448),fW[0][2],c.bW[0][0]); rd2(IC(64),IC(64+4*448),fW[0][3],c.bW[0][1]);
    rd2(IC(128),IC(128+4*448),fW[0][4],c.bW[0][0]); rd2(IC(128),IC(128+4*448),fW[0][5],c.bW[0][1]);
    rd2(IC(0),IC(4*448),fW[0][6],c.bW6[0]);
    waitl<0>(); }
  bld32_sc1<0>(pb,vF,srdF,0u); bld32_sc1<0>(pc,vF,srdF,(unsigned)(64*FST*4));
  tileH(IC(2)); tileW(IC(3));
  it12<0,true,true,8>(c,acc,fH,fW);
  waitv<10>();
  pin(pb); pin(pc);
  if(w==0 && !__all(pb==epoch && pc==epoch)){
    const unsigned* f0p=flags+(size_t)(FLG_LN+l)*FST; const unsigned* f1p=f0p+64*FST;
    for(unsigned spins=0; spins<(1u<<22); ++spins){ const unsigned a=flag_ld(f0p), b=flag_ld(f1p); if(__all(a==epoch && b==epoch)) break; __builtin_amdgcn_s_sleep(1); } }
  it12<1,true,true,10>(c,acc,fH,fW);
  for(int t=2;t<26;t+=12){
    it12<2,true,true,10>(c,acc,fH,fW); it12<3,true,true,10>(c,acc,fH,fW);
    it12<4,true,true,10>(c,acc,fH,fW); it12<5,true,true,10>(c,acc,fH,fW); it12<6,true,true,10>(c,acc,fH,fW); it12<7,true,true,10>(c,acc,fH,fW);
    it12<8,true,true,10>(c,acc,fH,fW); it12<9,true,true,10>(c,acc,fH,fW); it12<10,true,true,10>(c,acc,fH,fW); it12<11,true,true,10>(c,acc,fH,fW);
    it12<12,true,true,10>(c,acc,fH,fW); it12<13,true,true,10>(c,acc,fH,fW); }
  it12<2,true,true,10>(c,acc,fH,fW);
  auto w2touch=[&](auto pc){ constexpr int P=decltype(pc)::value;
    { const i32x4 srd2=make_srd(p.w2,F*S*2); const int lo_=otid()&63; const unsigned v16=(lo_<16)? (unsigned)(((mi*8+w)*16+lo_)*128) : 0x80000000u;
      asm volatile("s_nop 4");
      if constexpr(P==0){ touch<0>(dT,v16,srd2,0u); touch<0>(dT,v16,srd2,2u*16384*128);
        touch<0>(dT,v16,srd2,5u*16384*128); }
      else touch<0>(dT,v16,srd2,6u*16384*128); }
    if constexpr(P==1 && C3PF>0) c3_touch(p,w,dT); };
  constexpr int NTE=3;
  it12<3,true,true,10>(c,acc,fH,fW);
  it12<4,true,false,10>(c,acc,fH,fW);
  w2touch(IC(0));
  it12<5,false,false,6+NTE>(c,acc,fH,fW);
  it12<6,false,false,0+NTE>(c,acc,fH,fW);
  w2touch(IC(1));
  { const int l_=otid()&63; last(acc,fH,fW,lds0+w*16384+l_*16,(unsigned)(kg*2048)); }
  asm volatile("s_nop 7" ::: "memory");
  #pragma unroll
  for(int a=0;a<7;a++)
  #pragma unroll
  for(int b=0;b<4;b++) pin(acc[a][b]);
  epilogue(p,(LAS char*)smem,acc,w,dt,ft);
  asm volatile("s_waitcnt vmcnt(7)" : "+v"(dT) :: "memory");
  return epoch;
}
}

namespace fin {
constexpr int PART=16384, SLOT=2*PART, NS=4, GB_OFF=NS*SLOT, LDS_BYTES=GB_OFF+2048;
constexpr int NB0=NB0X;
struct Src { i32x4 srd; unsigned v[2]; unsigned soff; };
struct Bases { unsigned aR[2], aR2[2], aT[2][4]; };
template<int SET,int SL,bool READ,bool RPA,int DMAK  ,int VM>
DI void iter(const Bases& RB, unsigned mS, unsigned mD, Src& sS, Src& sD, f32x4 (&acc)[4][4], u32x4 (&fS)[2][4], u32x4 (&fD)[2][4]){
  constexpr int NX=SET^1, RS=(SL+1)&3, DS=SL;
  constexpr int RHI=RS>>1, RO=(RS&1)*SLOT;
  if constexpr(VM>=0){ waitv<VM>(); bar(); }
  u32x2 tl[4],th[4];
  auto RR=[&](auto ic){ constexpr int i=decltype(ic)::value;
    if constexpr(READ){ if constexpr(RPA) ds_b128<RO+i*2048>(fS[NX][i],RB.aR[RHI]); else ds_b128<RO+PART+i*2048>(fD[NX][i],(i<2)? RB.aR[RHI] : RB.aR2[RHI]); } };
  auto RT=[&](auto ic,auto hc){ constexpr int i=decltype(ic)::value,h=decltype(hc)::value;
    if constexpr(READ){
      if constexpr(RPA){ if constexpr(h==0) ds_tr<RO+PART>(tl[i],RB.aT[RHI][i]); else { ds_tr<RO+PART+1024>(th[i],RB.aT[RHI][i]); fD[NX][i]=cat(tl[i],th[i]); } }
      else { if constexpr(h==0) ds_tr<RO>(tl[i],RB.aT[RHI][i]); else { ds_tr<RO+1024>(th[i],RB.aT[RHI][i]); fS[NX][i]=cat(tl[i],th[i]); } } } };
  auto MS=[&](){ if constexpr(DMAK!=0) set_m0<DS*SLOT>(mS); };
  auto MD=[&](){ if constexpr(DMAK!=0) set_m0<DS*SLOT>(mD); };
  auto DSs=[&](auto kc){ constexpr int k=decltype(kc)::value; if constexpr(DMAK!=0) dma<k*1024,(DMAK==2)?0:2>(sS.v[k],sS.srd,sS.soff); };
  auto DDd=[&](auto kc){ constexpr int k=decltype(kc)::value; if constexpr(DMAK!=0) dma<k*1024,(DMAK==1)?0:2>(sD.v[k],sD.srd,sD.soff); };
  auto MM=[&](auto ac,auto bc){ constexpr int a=decltype(ac)::value,b=(a&1)? 3-decltype(bc)::value : decltype(bc)::value; mfma(acc[a][b],fD[SET][a],fS[SET][b]); };
  auto M1=[&](){ if constexpr(DMAK==1) MS(); else MD(); };
  auto M2=[&](){ if constexpr(DMAK==1) MD(); else MS(); };
  auto D1=[&](auto kc){ if constexpr(DMAK==1) DSs(kc); else DDd(kc); };
  auto D2=[&](auto kc){ if constexpr(DMAK==1) DDd(kc); else DSs(kc); };
  auto G=[&](auto gc){ constexpr int g=decltype(gc)::value;
    if constexpr(g==FG0-1) M1();
    if constexpr(g==FG0) D1(IC(0));
    if constexpr(g==FG0+FSP) D1(IC(1));
    if constexpr(g==FG0+FSP+1) M2();
    if constexpr(g==FG0+2*FSP) D2(IC(0));
    if constexpr(g==FG0+3*FSP) D2(IC(1));
    if constexpr(g==15){ if constexpr(DMAK==1) saddi2<262144,64*2048>(sS.soff,sD.soff); if constexpr(DMAK==2) saddi2<64*4096,16384>(sS.soff,sD.soff); } };
  MM(IC(0),IC(0)); RR(IC(0)); G(IC(0));
  MM(IC(0),IC(1)); RT(IC(0),IC(0)); G(IC(1));
  MM(IC(0),IC(2)); RT(IC(0),IC(1)); G(IC(2));
  MM(IC(0),IC(3)); RR(IC(1)); G(IC(3));
  MM(IC(1),IC(0)); RT(IC(1),IC(0)); G(IC(4));
  MM(IC(1),IC(1)); RT(IC(1),IC(1)); G(IC(5));
  MM(IC(1),IC(2)); RR(IC(2)); G(IC(6));
  MM(IC(1),IC(3)); RT(IC(2),IC(0)); G(IC(7));
  MM(IC(2),IC(0)); RT(IC(2),IC(1)); G(IC(8));
  MM(IC(2),IC(1)); RR(IC(3)); G(IC(9));
  MM(IC(2),IC(2)); RT(IC(3),IC(0)); G(IC(10));
  MM(IC(2),IC(3)); RT(IC(3),IC(1)); G(IC(11));
  MM(IC(3),IC(0)); G(IC(12)); MM(IC(3),IC(1)); G(IC(13)); MM(IC(3),IC(2)); G(IC(14)); MM(IC(3),IC(3)); G(IC(15));
  waitl0();
}
template<bool PA> DI void mk_bases(Bases& B, unsigned lds0, int wsd, int wd, int kg){
  const int l=otid()&63, g=l>>4,q=(l&15)>>2,pp=l&3,i16=l&15;
  #pragma unroll
  for(int hi=0;hi<2;hi++){
    const unsigned base=lds0+hi*2*SLOT;
    const unsigned ar=base+(64*(PA?wsd:wd)+i16)*128+(((4*kg+g)^((i16>>1)&7))*16);
    const int pk=kg;
    B.aR[hi]=ar+(PA? 0 : pk*4096); B.aR2[hi]=ar-(PA? 0 : pk*4096);
    const int row=32*kg+8*g+q; const int sw=q|((g&1)<<2);
    #pragma unroll
    for(int e=0;e<4;e++) B.aT[hi][e]=base+row*256+(((4*(PA?wd:wsd)+(PA? e^(2*pk) : e))^sw)*32)+8*pp;
  }
}
template<bool PA> DI void mk_src(const Params& p, Src& sS, Src& sD, int w, int s0, int dt, int half){
  const int l=otid()&63;
  #pragma unroll
  for(int k=0;k<2;k++){
    const int rowR=16*w+8*k+(l>>3); const int chR=(l&7)^((rowR>>1)&7);
    const int rowT=8*w+4*k+(l>>4); const int sw=(rowT&3)|(((rowT>>3)&1)<<2); const int pos=l&15; const int chT=(((pos>>1)^sw)<<1)|(pos&1);
    if constexpr(PA){ sS.v[k]=(chR>>2)*131072+(s0+rowR)*64+(chR&3)*16-1024*k; sD.v[k]=rowT*2048+dt*256+chT*16-1024*k; }
    else            { sS.v[k]=rowT*4096+s0*2+chT*16-1024*k; sD.v[k]=dt*112*8192+(chR>>2)*8192+rowR*64+(chR&3)*16-1024*k; }
  }
  if constexpr(PA){ sS.srd=make_srd(p.ws+WS_O,S*D*2); sD.srd=make_srd(p.wo,D*D*2); sS.soff=0; sD.soff=0; }
  else { sS.srd=make_srd(p.w2,F*S*2); sD.srd=make_srd(p.ws+WS_U,D*F*2); sS.soff=rfl(half? NB0*64*4096 : 0); sD.soff=rfl(half? NB0*16384 : 0); }
}
DI void pinf(float& x){ asm volatile("" : "+v"(x)); }
DI f32x4 mix(const f32x4& a,const f32x4& b,float g,float h){
  const f32x2 g2=bc2(g), h2=bc2(h);
  const f32x2 lo=fma2(lo2(b),h2,lo2(a)*g2), hi=fma2(hi2(b),h2,hi2(a)*g2); return __builtin_shufflevector(lo,hi,0,1,2,3); }
template<int HALF,int SET>
DI void last(f32x4 (&acc)[4][4], f32x4 (&accB)[4][4], const float (&gt)[4], u32x4 (&fS)[2][4], u32x4 (&fD)[2][4], unsigned wa, unsigned ra, f32x4 (&r)[2][4]){
  constexpr int SA=2, KA=0;
  auto MM=[&](auto ac,auto bc){ constexpr int a=decltype(ac)::value,b=decltype(bc)::value; mfma(acc[a][b],fD[SET][a],fS[SET][b]); };
  auto val=[&](auto ac,auto bc){ constexpr int a=decltype(ac)::value,b=decltype(bc)::value;
    pin(acc[a][b]);
    if constexpr(HALF==0) return mix(acc[a][b],accB[a][b],gt[b],1.0f-gt[b]); else return acc[a][b]; };
  auto SND=[&](auto ic){ constexpr int i=decltype(ic)::value, a=i>>2, b=i&3; ds_w128<(b*2+a)*1024>(wa, val(IC(SA+a),IC(b))); };
  MM(IC(SA),IC(0)); MM(IC(SA),IC(1)); MM(IC(SA),IC(2)); MM(IC(SA),IC(3)); MM(IC(SA+1),IC(0)); MM(IC(SA+1),IC(1)); MM(IC(SA+1),IC(2)); MM(IC(SA+1),IC(3));
  MM(IC(KA),IC(0)); MM(IC(KA),IC(1)); asm volatile("s_nop 2");
  SND(IC(0)); MM(IC(KA),IC(2)); SND(IC(1)); MM(IC(KA),IC(3)); SND(IC(2)); MM(IC(KA+1),IC(0)); SND(IC(3)); MM(IC(KA+1),IC(1));
  SND(IC(4)); MM(IC(KA+1),IC(2)); SND(IC(5)); MM(IC(KA+1),IC(3)); SND(IC(6)); SND(IC(7));
  asm volatile("s_nop 7");
  f32x4 k[2][4];
  k[0][0]=val(IC(KA),IC(0)); k[0][1]=val(IC(KA),IC(1)); k[0][2]=val(IC(KA),IC(2)); k[0][3]=val(IC(KA),IC(3));
  k[1][0]=val(IC(KA+1),IC(0)); k[1][1]=val(IC(KA+1),IC(1)); k[1][2]=val(IC(KA+1),IC(2)); k[1][3]=val(IC(KA+1),IC(3));
  #pragma unroll
  for(int b=0;b<4;b++){ pin(k[0][b]); pin(k[1][b]); }
  lbar();
  u32x4 v[2][4];
  ds_b128<0>(v[0][0],ra); ds_b128<1024>(v[1][0],ra); ds_b128<2048>(v[0][1],ra); ds_b128<3072>(v[1][1],ra);
  ds_b128<4096>(v[0][2],ra); ds_b128<5120>(v[1][2],ra); ds_b128<6144>(v[0][3],ra); ds_b128<7168>(v[1][3],ra);
  waitl<0>();
  #pragma unroll
  for(int b=0;b<4;b++){ pin(v[0][b]); pin(v[1][b]); r[0][b]=k[0][b]+__builtin_bit_cast(f32x4,v[0][b]); r[1][b]=k[1][b]+__builtin_bit_cast(f32x4,v[1][b]); }
}
template<int HALF>
DI void run(const Params& p, char* smem, int st, int dt, unsigned epoch, int myflag  ){
  const unsigned lds0=(unsigned)(size_t)(LAS char*)smem;
  LAS char* sm=(LAS char*)smem;
  const int w=rfl(threadIdx.x>>6);
  const int wsd=w&1, wd=(w>>1)&1, kg=w>>2;
  const int s0=st*128, d0=dt*128;
  const unsigned mS=rfl(lds0+w*2048), mD=rfl(lds0+PART+w*2048);
  unsigned* flags=(unsigned*)(p.ws+WS_FLG);
  f32x4 accA[4][4], accB[4][4]; u32x4 fS[2][4], fD[2][4];
  unsigned bz[4];
  if constexpr(HALF==1){ const int i16_=otid()&15; const u16* bp=p.b2+s0+64*wsd+i16_;
    #pragma unroll
    for(int b=0;b<4;b++) asm volatile("global_load_ushort %0, %1, off offset:%2" : "=v"(bz[b]) : "v"(bp), "i"(b*32) : "memory"); }
  auto load_gb=[&](){
    if(w==0){ const int l=otid()&63; i32x4 sg=make_srd(p.ws+WS_G,S*4); set_m0<0>(rfl(lds0+GB_OFF)); asm volatile("s_nop 4"); dma<0,2>((unsigned)(s0*4+l*16),sg,0u); } };
  auto tileS=[&](auto slc, auto pac, Src& sS){ constexpr int SLT=decltype(slc)::value; constexpr bool PA=decltype(pac)::value!=0;
    set_m0<SLT*SLOT>(mS); nop1(); dma<0,PA?2:0>(sS.v[0],sS.srd,sS.soff); dma<1024,PA?2:0>(sS.v[1],sS.srd,sS.soff);
    if constexpr(PA) saddi<262144>(sS.soff); else saddi<64*4096>(sS.soff); };
  auto tileD=[&](auto slc, auto pac, Src& sD){ constexpr int SLT=decltype(slc)::value; constexpr bool PA=decltype(pac)::value!=0;
    set_m0<SLT*SLOT>(mD); nop1(); dma<0,PA?0:2>(sD.v[0],sD.srd,sD.soff); dma<1024,PA?0:2>(sD.v[1],sD.srd,sD.soff);
    if constexpr(PA) saddi<64*2048>(sD.soff); else saddi<16384>(sD.soff); };
  auto first_read=[&](auto pac, const Bases& B){ constexpr bool PA=decltype(pac)::value!=0;
    u32x2 l0,h0,l1,h1,l2,h2,l3,h3;
    if constexpr(PA){
      ds_b128<0>(fS[0][0],B.aR[0]); ds_b128<2048>(fS[0][1],B.aR[0]); ds_b128<4096>(fS[0][2],B.aR[0]); ds_b128<6144>(fS[0][3],B.aR[0]);
      ds_tr<PART>(l0,B.aT[0][0]); ds_tr<PART+1024>(h0,B.aT[0][0]); ds_tr<PART>(l1,B.aT[0][1]); ds_tr<PART+1024>(h1,B.aT[0][1]);
      ds_tr<PART>(l2,B.aT[0][2]); ds_tr<PART+1024>(h2,B.aT[0][2]); ds_tr<PART>(l3,B.aT[0][3]); ds_tr<PART+1024>(h3,B.aT[0][3]);
      fD[0][0]=cat(l0,h0); fD[0][1]=cat(l1,h1); fD[0][2]=cat(l2,h2); fD[0][3]=cat(l3,h3);
    } else {
      ds_b128<PART>(fD[0][0],B.aR[0]); ds_b128<PART+2048>(fD[0][1],B.aR[0]); ds_b128<PART+4096>(fD[0][2],B.aR2[0]); ds_b128<PART+6144>(fD[0][3],B.aR2[0]);
      ds_tr<0>(l0,B.aT[0][0]); ds_tr<1024>(h0,B.aT[0][0]); ds_tr<0>(l1,B.aT[0][1]); ds_tr<1024>(h1,B.aT[0][1]);
      ds_tr<0>(l2,B.aT[0][2]); ds_tr<1024>(h2,B.aT[0][2]); ds_tr<0>(l3,B.aT[0][3]); ds_tr<1024>(h3,B.aT[0][3]);
      fS[0][0]=cat(l0,h0); fS[0][1]=cat(l1,h1); fS[0][2]=cat(l2,h2); fS[0][3]=cat(l3,h3);
    }
    waitl<0>(); };
  #define ITB(P,RD,DK,VM) iter<(P)&1,(P)&3,RD,false,DK,VM>(Bb,mS,mD,bS,bD,accB,fS,fD)
  #define ITX(P,RPA,DK,VM,RB,AC) iter<(P)&1,(P)&3,true,RPA,DK,VM>(RB,mS,mD,aS,aD,AC,fS,fD)
  constexpr int K0=HALF? NB0 : 0, NT=HALF? 56-NB0 : NB0;
  constexpr int PLO=(K0*64)/224;
  constexpr int NL=((NT-4)/4)*4, R=(NT-4)&3;
  Bases Bb; Src bS,bD;
  mk_src<false>(p,bS,bD,w,s0,dt,HALF); mk_bases<false>(Bb,lds0,wsd,wd,kg);
  waitv<0>(); bar();
  if(threadIdx.x==0) flag_st(flags+myflag, epoch);
  tileS(IC(0),IC(0),bS); tileS(IC(1),IC(0),bS); tileS(IC(2),IC(0),bS); tileS(IC(3),IC(0),bS);
  const unsigned* uline=flags+PFLG_U+dt*32;
  auto upto=[&](int kt){ int ph=(kt*64+63)/224; ph=ph>15?15:ph; return ((2u<<ph)-1u)&~((1u<<PLO)-1u); };
  if(w==0) poll_line(uline, upto(K0+7), epoch, otid()&63);
  bar();
  load_gb();
  tileD(IC(0),IC(0),bD); tileD(IC(1),IC(0),bD); tileD(IC(2),IC(0),bD);
  #pragma unroll
  for(int b=0;b<4;b++){ float z=0.f; if constexpr(HALF==1){ pin(bz[b]); z= kg? 0.f : __uint_as_float(bz[b]<<16); }
    #pragma unroll
    for(int a=0;a<4;a++){ accB[a][b]=(f32x4){z,z,z,z}; pin(accB[a][b]); if constexpr(HALF==0){ accA[a][b]=(f32x4){0,0,0,0}; pin(accA[a][b]); } } }
  waitv<4>(); bar(); first_read(IC(0),Bb);
  tileD(IC(3),IC(0),bD);
  float gt[4];
  { const int i16_=otid()&15;
    #pragma unroll
    for(int b=0;b<4;b++){ gt[b]=*(LAS float*)(sm+GB_OFF+(64*wsd+16*b+i16_)*4); pinf(gt[b]); } }
  unsigned fv, pok=0;
  const unsigned* oline=flags+PFLG_O+(st>>1)*32;
  const unsigned* fl=(HALF==0 && (otid()&16))? oline+(otid()&15)*2 : uline+(otid()&15)*2;
  for(int t=0;t<NL;t+=4){
    gld32_sc1(fv, fl);
    if(t==0) waitv<5>();
    ITB(0,true,2,9);
    if(t==0) waitv<7>();
    ITB(1,true,2,9); ITB(2,true,2,9); ITB(3,true,2,8);
    pin(fv);
    const unsigned need=upto(t+11<NT? K0+t+11 : K0+NT-1);
    const unsigned seen=(unsigned)__ballot(fv==epoch);
    if constexpr(HALF==0) pok|=((seen>>16)==0xffffu);
    if((seen&need)!=need) poll_line(uline, need, epoch, otid()&63);
  }
  if constexpr(R==2){
    if constexpr(HALF==0) gld32_sc1(fv, fl);
    ITB(0,true,2,8); ITB(1,true,2,8);
    if constexpr(HALF==0){ waitv<8>(); pin(fv); pok|=(((unsigned)__ballot(fv==epoch)>>16)==0xffffu); } }
  unsigned xf=0;
  if constexpr(HALF==0){
    Bases Ba; Src aS,aD;
    mk_src<true>(p,aS,aD,w,s0,dt,0);
    if(!pok) poll_line(oline, 0xffffu, epoch, otid()&63);
    ITX(R,false,1,8,Bb,accB); ITX(R+1,false,1,8,Bb,accB); ITX(R+2,false,1,8,Bb,accB);
    mk_bases<true>(Ba,lds0,wsd,wd,kg);
    ITX(R+3,true,1,8,Ba,accB);
    for(int t=0;t<12;t+=4){ ITX(R,true,1,8,Ba,accA); ITX(R+1,true,1,8,Ba,accA); ITX(R+2,true,1,8,Ba,accA); ITX(R+3,true,1,8,Ba,accA); }
    ITX(R,true,0,8,Ba,accA); ITX(R+1,true,0,4,Ba,accA); ITX(R+2,true,0,0,Ba,accA);
  } else {
    ITB(R,true,0,8); ITB(R+1,true,0,4); ITB(R+2,true,0,0);
    gld32_sc1(xf, flags+(FLG_X+st*8+dt)*FST+w*2);
  }
  {
    const int tid=otid(), l=tid&63, g=l>>4, i16=l&15;
    const unsigned wa=lds0+R*SLOT+w*8192+l*16, ra=lds0+R*SLOT+(w^4)*8192+l*16;
    f32x4 r[2][4];
    if constexpr(HALF==0) last<0,(R+3)&1>(accA,accB,gt,fS,fD,wa,ra,r); else last<1,(R+3)&1>(accB,accB,gt,fS,fD,wa,ra,r);
    #pragma unroll
    for(int b=0;b<4;b++){ pin(r[0][b]); pin(r[1][b]); }
    const __amdgpu_buffer_rsrc_t rX=make_rsrc(p.ws+WS_XCH,128*32768);
    const unsigned xoff=(unsigned)((st*8+dt)*32768+w*4096+l*16);
    unsigned* xflag=flags+(FLG_X+st*8+dt)*FST+w*2;
    if constexpr(HALF==0){
      #pragma unroll
      for(int b=0;b<4;b++) st_sc1(rX,xoff+b*1024,(u32x4){cvtpk(lo2(r[0][b])),cvtpk(hi2(r[0][b])),cvtpk(lo2(r[1][b])),cvtpk(hi2(r[1][b]))});
      asm volatile("s_waitcnt vmcnt(0)" ::: "memory");
      if(l==0) flag_st(xflag, epoch);
    } else {
      const __amdgpu_buffer_rsrc_t rOut=make_rsrc(p.out,S*D*2);
      unsigned ooff=(unsigned)(((s0+64*wsd+(l>>2))*D+d0+64*wd+32*kg)*2+(l&3)*16); pin(ooff);
      asm volatile("s_waitcnt vmcnt(0)" : "+v"(xf) :: "memory");
      if(!__all(xf==epoch)){ for(unsigned spins=0; spins<(1u<<22); ++spins){ if(__all(flag_ld(xflag)==epoch)) break; __builtin_amdgcn_s_sleep(1); } }
      u32x4 t[4];
      #pragma unroll
      for(int b=0;b<4;b++) t[b]=ld_sc1(rX,xoff+b*1024);
      const int offg=(g&1)*16+(g>>1)*8;
      LAS char* tb=sm+((R+2)&3)*SLOT+w*4096;
      #pragma unroll
      for(int b=0;b<4;b++){
        const f32x2 h2=bc2(1.0f-gt[b]);
        const f32x2 x0l=fma2(lo2(r[0][b]),h2,bf2(t[b][0])), x0h=fma2(hi2(r[0][b]),h2,bf2(t[b][1])), x1l=fma2(lo2(r[1][b]),h2,bf2(t[b][2])), x1h=fma2(hi2(r[1][b]),h2,bf2(t[b][3]));
        const pr2 r0=sw16(cvtpk(x0l),cvtpk(x1l)), r1=sw16(cvtpk(x0h),cvtpk(x1h));
        *(LAS u32x4*)(tb+b*1024+i16*64+offg*2)=(u32x4){r0[0],r1[0],r0[1],r1[1]};
      }
      #pragma unroll
      for(int b=0;b<4;b++) __builtin_amdgcn_raw_buffer_store_b128(*(LAS u32x4*)(tb+b*1024+l*16), rOut, ooff, b*16*D*2, 16);
    }
  }
}
}

__global__ __launch_bounds__(512,2) void k_fused(Params p){
  extern __shared__ __attribute__((aligned(16))) char smem[];
  const int b=blockIdx.x, xcd=b&7, j=b>>3;
  { unsigned lo,hi,sz;
    asm volatile("s_getpc_b64 s[98:99]\n.Lpc0_%=:\n\ts_sub_u32 s98, s98, .Lpc0_%=-_Z7k_fused6Params\n\ts_subb_u32 s99, s99, 0\n\ts_mov_b32 %0, s98\n\ts_mov_b32 %1, s99\n\ts_mov_b32 %2, k_code_end-_Z7k_fused6Params" : "=s"(lo), "=s"(hi), "=s"(sz) :: "s98", "s99", "scc");
    i32x4 sc; sc[0]=(int)lo; sc[1]=(int)(hi&0xffff); sc[2]=(int)sz; sc[3]=0x00020000;
    const int w_=rfl(threadIdx.x>>6), l_=threadIdx.x&63;
    const bool q_=(j&1)==0;
    const unsigned lds0=(unsigned)(size_t)(LAS char*)smem;
    const unsigned lb=q_? qkv::WPHI_OFF+8192 : mix1::WOFF+3*mix1::WSLOT, ls=q_? 256 : 3584;
    set_m0<0>(rfl(lds0+lb+w_*ls));
    const unsigned vo=(l_<2)? (unsigned)(((j*8+w_)*2+l_)*128) : 0x80000000u;
    asm volatile("s_nop 4\n\tbuffer_load_dword %0, %1, 0 offen lds" :: "v"(vo), "s"(sc) : "memory"); }
  const int jj=j>>1;
  const bool isq=(j&1)==0;
  const int pc=2*xcd+(jj>>3), pr=jj&7, mi=xcd*16+jj;
  const int st=((xcd&3)&1)*8+(j&7), dtf=((xcd&3)>>1)*4+(j>>3);
  const bool h0=xcd<4;
  unsigned epoch; int myflag;
  const int qc=pc&15;
  if(isq){ epoch=qkv::run(p,smem,pr,qc); myflag=PFLG_O+(pr*16+qc)*2; } else { epoch=mix1::run(p,smem,pr,pc,mi); myflag=PFLG_U+(pr*16+pc)*2; }
  if(h0) fin::run<0>(p,smem,st,dtf,epoch,myflag); else fin::run<1>(p,smem,st,dtf,epoch,myflag);
  if(b==0 && threadIdx.x==0) flag_st((unsigned*)(p.ws+WS_FLG), epoch);
}

extern "C" __device__ __attribute__((noinline,used)) void k_code_end(){ }

extern "C" void kernel_launch(void* const* d_in, const int* in_sizes, int n_in, void* d_out, int out_size, void* d_ws, size_t ws_size, hipStream_t stream){
  (void)in_sizes; (void)n_in; (void)out_size; (void)ws_size;
  Params p;
  p.x=(const u16*)d_in[0]; p.wqkv=(const u16*)d_in[1]; p.wphi=(const u16*)d_in[2]; p.wo=(const u16*)d_in[3];
  p.w1=(const u16*)d_in[5]; p.b1=(const u16*)d_in[6]; p.w2=(const u16*)d_in[7]; p.b2=(const u16*)d_in[8]; p.gw=(const u16*)d_in[9];
  p.out=(u16*)d_out; p.ws=(char*)d_ws;
  constexpr int L1_=(qkv::LDS_BYTES>mix1::LDS_BYTES)?qkv::LDS_BYTES:mix1::LDS_BYTES;
  constexpr int LDS_ALL=(L1_>fin::LDS_BYTES)?L1_:fin::LDS_BYTES;
  (void)hipFuncSetAttribute((const void*)k_fused, hipFuncAttributeMaxDynamicSharedMemorySize, LDS_ALL);
  k_fused<<<256,512,LDS_ALL,stream>>>(p);
}
```
